```python
import torch

def _softmax(x, axis=-1):
    return torch.softmax(x, dim=axis)
def _gelu(x):
    return torch.nn.functional.gelu(x, approximate="tanh")
def _silu(x):
    return torch.nn.functional.silu(x)
def _sigmoid(x):
    return torch.sigmoid(x)
def _layernorm(x, axis=-1, eps=1e-5):
    m = x.mean(dim=axis, keepdim=True)
    v = x.var(dim=axis, keepdim=True, unbiased=False)
    return (x - m) / torch.sqrt(v + eps)
def _scatter_add(tgt, idx, src, axis=0):
    return tgt.clone().index_add_(axis, idx, src)
def _segment_sum(src, seg, n):
    out = torch.zeros((n,) + src.shape[1:], dtype=src.dtype)
    return out.index_add_(0, seg, src)
def _pad(x, pw):
    flat = tuple(v for pair in reversed(pw) for v in pair)
    return torch.nn.functional.pad(x, flat)

kernel_name = 'standalone__gqa_block_cached__sampling'

def setup_inputs():

    _w = (lambda a: (lambda t: t.to(torch.bfloat16) if t.is_floating_point() else t)(torch.as_tensor(a)))
    B, Sq, D = 32, 4, 576
    Hq, Hkv, Sk, Dh = 32, 16, 2048, 128
    assert Hq % Hkv == 0, (Hq, Hkv)
    return {
        "x": _w((lambda *s: torch.randn(*s, dtype=torch.bfloat16) * 0.8)(B, Sq, D)),
        "k_cache": _w((lambda *s: torch.randn(*s, dtype=torch.bfloat16) * 0.8)(B, Hkv, Sk, Dh)),
        "v_cache": _w((lambda *s: torch.randn(*s, dtype=torch.bfloat16) * 0.05)(B, Hkv, Sk, Dh)),
        "wq": _w((lambda *s: torch.randn(*s, dtype=torch.bfloat16) * 0.8)(D, Hq * Dh) * (D ** -0.5)),
        "wo": _w((lambda *s: torch.randn(*s, dtype=torch.bfloat16) * 0.05)(Hq * Dh, D) * ((Hq * Dh) ** -0.5)),
    }

def reference(x, k_cache, v_cache, wq, wo):
    B, Sq, D = x.shape
    Hq, Dh = 32, 128
    Hkv = k_cache.shape[1]
    rep = Hq // Hkv
    q = (lambda x, *d: x.permute(*d))((x @ wq).reshape(B, Sq, Hq, Dh), 0, 2, 1, 3)
    k = (lambda x, n, axis: torch.repeat_interleave(x, n, dim=axis))(k_cache, rep, axis=1)
    v = (lambda x, n, axis: torch.repeat_interleave(x, n, dim=axis))(v_cache, rep, axis=1)
    scores = torch.einsum("bhqd,bhkd->bhqk", q, k) * (Dh ** -0.5)
    p = _softmax(scores, axis=-1)
    o = torch.einsum("bhqk,bhkd->bhqd", p, v)
    return (lambda x, *d: x.permute(*d))(o, 0, 2, 1, 3).reshape(B, Sq, Hq * Dh) @ wo
```

```cpp
#include <hip/hip_runtime.h>
#include <stdint.h>
#include <type_traits>

namespace gqa {
typedef __bf16 bf16x8 __attribute__((ext_vector_type(8)));
typedef __bf16 bf16x4 __attribute__((ext_vector_type(4)));
typedef __bf16 bf16x2 __attribute__((ext_vector_type(2)));
typedef float f32x4 __attribute__((ext_vector_type(4)));
typedef unsigned u32x4 __attribute__((ext_vector_type(4)));
typedef unsigned u32x2 __attribute__((ext_vector_type(2)));
#define LAS __attribute__((address_space(3)))

constexpr int SQ = 4, DM = 576, HKV = 16, SK = 2048, DH = 128, NQ = 4096;
#define INFL 4
#define KDELAY -1
#define PSLEEP 32
#define PDELAY 12
#define TAU16 440
#define TORG 330
constexpr int TILE = 8192, NTILE = 256;
constexpr int NSLOT_A = 17, NSLOT_B = 9, N_SW = 128;
constexpr unsigned L_W = 4 * TILE, L_X = 13 * TILE;
constexpr unsigned L_PARK = NSLOT_B * TILE;
constexpr int N_FREE0 = 4;
constexpr unsigned L_SCR = NSLOT_A * TILE, SCR_PITCH = 528, SCR_W = 8 * SCR_PITCH;
constexpr unsigned QO_PITCH = 272;
constexpr unsigned L_QO = L_SCR + 17408;
constexpr unsigned L_ML = L_QO + 16 * QO_PITCH;
constexpr unsigned L_FLG = L_ML + 256;
constexpr unsigned LDS_BYTES = L_FLG + 128;
constexpr float C2 = 0.08838834764831845f * 1.4426950408889634f;
constexpr size_t WS_EQ = 0, WS_EO = 64 * 128, WS_QM = 65536, WS_OM = 4u << 20;
constexpr unsigned QM_BYTES = 256 * 8192, OM_STRIDE = 2304 * 16, OM_BYTES = 256 * OM_STRIDE;
constexpr long long SPIN_CAP = 2000000;

__device__ __forceinline__ unsigned lds_ld(unsigned addr) {
  unsigned v; asm volatile("ds_read_b32 %0, %1\n\ts_waitcnt lgkmcnt(0)" : "=v"(v) : "v"(addr) : "memory");
  return __builtin_amdgcn_readfirstlane(v);
}
__device__ __forceinline__ u32x4 lds_ld4(unsigned addr) {
  u32x4 v; asm volatile("ds_read_b128 %0, %1\n\ts_waitcnt lgkmcnt(0)" : "=v"(v) : "v"(addr) : "memory");
  u32x4 r; r[0] = __builtin_amdgcn_readfirstlane(v[0]); r[1] = __builtin_amdgcn_readfirstlane(v[1]);
  r[2] = __builtin_amdgcn_readfirstlane(v[2]); r[3] = __builtin_amdgcn_readfirstlane(v[3]); return r;
}
__device__ __forceinline__ void lds_st(unsigned addr, unsigned val) {
  asm volatile("ds_write_b32 %0, %1" :: "v"(addr), "v"(val) : "memory");
}
__device__ __forceinline__ void lds_drain() { asm volatile("s_waitcnt lgkmcnt(0)" ::: "memory"); }
__device__ __forceinline__ void dma8(unsigned o0, unsigned o1, unsigned o2, unsigned o3, const char* src, const char* src2, unsigned lds) {
  asm volatile("s_mov_b32 m0, %6\n\ts_nop 0\n\t"
               "global_load_lds_dwordx4 %0, %4 nt\n\t"
               "global_load_lds_dwordx4 %1, %4 offset:1024 nt\n\t"
               "global_load_lds_dwordx4 %2, %4 offset:2048 nt\n\t"
               "global_load_lds_dwordx4 %3, %4 offset:3072 nt\n\t"
               "s_add_u32 m0, m0, 0x1000\n\ts_nop 0\n\t"
               "global_load_lds_dwordx4 %0, %5 nt\n\t"
               "global_load_lds_dwordx4 %1, %5 offset:1024 nt\n\t"
               "global_load_lds_dwordx4 %2, %5 offset:2048 nt\n\t"
               "global_load_lds_dwordx4 %3, %5 offset:3072 nt"
               :: "v"(o0), "v"(o1), "v"(o2), "v"(o3), "s"(src), "s"(src2), "s"(lds) : "memory", "scc");
}
__device__ __forceinline__ void dma1(unsigned o, const char* src, unsigned lds) {
  asm volatile("s_mov_b32 m0, %2\n\ts_nop 0\n\tglobal_load_lds_dwordx4 %0, %1 nt" :: "v"(o), "s"(src), "s"(lds) : "memory");
}
template <int N> __device__ __forceinline__ void wait_vm() { asm volatile("s_waitcnt vmcnt(%0)" :: "n"(N) : "memory"); }
template <int CTRL> __device__ __forceinline__ float dppf(float x) {
  return __builtin_bit_cast(float, __builtin_amdgcn_mov_dpp(__builtin_bit_cast(int, x), CTRL, 0xf, 0xf, true));
}
__device__ __forceinline__ float xrow_max(float x) {
  auto s = __builtin_amdgcn_permlane16_swap(__float_as_uint(x), __float_as_uint(x), false, false);
  x = fmaxf(__uint_as_float(s[0]), __uint_as_float(s[1]));
  auto t = __builtin_amdgcn_permlane32_swap(__float_as_uint(x), __float_as_uint(x), false, false);
  return fmaxf(__uint_as_float(t[0]), __uint_as_float(t[1]));
}
__device__ __forceinline__ float xrow_sum(float x) {
  auto s = __builtin_amdgcn_permlane16_swap(__float_as_uint(x), __float_as_uint(x), false, false);
  x = __uint_as_float(s[0]) + __uint_as_float(s[1]);
  auto t = __builtin_amdgcn_permlane32_swap(__float_as_uint(x), __float_as_uint(x), false, false);
  return __uint_as_float(t[0]) + __uint_as_float(t[1]);
}
__device__ __forceinline__ unsigned read_epoch(const char* p) {
  unsigned e; asm volatile("s_load_dword %0, %1, 0x0\n\ts_waitcnt lgkmcnt(0)" : "=s"(e) : "s"(p) : "memory"); return e;
}
__device__ __forceinline__ bool all_ge(const u32x4 v, unsigned k) { return v[0] >= k && v[1] >= k && v[2] >= k && v[3] >= k; }
#define MFMA(a, b, c) __builtin_amdgcn_mfma_f32_16x16x32_bf16((a), (b), (c), 0, 0, 0)
#define TRREAD(p) __builtin_amdgcn_ds_read_tr16_b64_v4bf16((LAS bf16x4*)(p))
#define CAT8(lo, hi) __builtin_shufflevector((lo), (hi), 0, 1, 2, 3, 4, 5, 6, 7)

constexpr unsigned LDS_TOTAL = LDS_BYTES;
#define TS(slot) do {} while (0)
#define TS_INIT() do {} while (0)
#define TS_DUMP() do {} while (0)

template <int NM>
__device__ __forceinline__ void out_stage(LAS char* L, unsigned lb, const bf16x8 (&WF)[8][NM], int col0, int wave, int lane, int bid, int kvh, int bp,
                                          char* __restrict__ ws, char* __restrict__ out) {
  const int n16 = lane & 15, g = lane >> 4;
  const unsigned eo = lds_ld(lb + L_FLG + 8);
  const unsigned T1 = 2 * eo + 3, T2 = T1 * 0x9E3779B1u;
  f32x4 acc[NM];
#pragma unroll
  for (int mt = 0; mt < NM; ++mt) acc[mt] = f32x4{0.f, 0.f, 0.f, 0.f};
  LAS char* ob = L + L_QO + ((n16 >> 2) & 1) * 8 * QO_PITCH + (n16 & 3) * QO_PITCH + g * 8;
  bf16x8 B[8];
#pragma unroll
  for (int ks = 0; ks < 8; ++ks) {
    LAS char* a = ob + (ks >> 2) * 4 * QO_PITCH + (ks & 3) * 64;
    B[ks] = CAT8(*(LAS bf16x4*)a, *(LAS bf16x4*)(a + 32));
  }
#pragma unroll
  for (int ks = 0; ks < 8; ++ks)
#pragma unroll
    for (int mt = 0; mt < NM; ++mt) acc[mt] = MFMA(WF[ks][mt], B[ks], acc[mt]);
  asm volatile("" :: "v"(acc[0]), "v"(acc[NM - 1]));
  TS(24);
  const __amdgpu_buffer_rsrc_t ro = __builtin_amdgcn_make_buffer_rsrc(ws + WS_OM, 0, OM_BYTES, 0x00020000);
  if (n16 < 8) {
#pragma unroll
    for (int mt = 0; mt < NM; ++mt) {
      const int cc = col0 + 16 * mt + 4 * g, j = cc / 36, q = (cc - 36 * j) >> 2;
      const int dbid = (bp << 4) | (j >> 1) | ((j & 1) << 3);
      const unsigned off = dbid * OM_STRIDE + (((kvh * 9 + q) * 2) * 8 + n16) * 16;
      const u32x4 a = __builtin_bit_cast(u32x4, acc[mt]);
      __builtin_amdgcn_raw_buffer_store_b128(u32x4{a[0], a[1], a[0] ^ T1, a[1] ^ T2}, ro, off, 0, 16);
      __builtin_amdgcn_raw_buffer_store_b128(u32x4{a[2], a[3], a[2] ^ T1, a[3] ^ T2}, ro, off + 128, 0, 16);
    }
  }
  TS(25);
  const int t = wave * 64 + lane;
  if (wave < 5) {
    int npoll = 0;
    const bool act = t < 288;
    const int idx = act ? (t >> 1) : 0;
    const unsigned off = bid * OM_STRIDE + ((t & 1) * 8 * 144 + idx) * 16;
    u32x4 v[8];
    unsigned miss = 0xff;
    const unsigned long long t0 = __builtin_amdgcn_s_memrealtime();
    for (;;) {
      asm volatile("" ::: "memory");
#pragma unroll
      for (int s = 0; s < 8; ++s) if (miss & (1u << s)) v[s] = __builtin_amdgcn_raw_buffer_load_b128(ro, off + s * 144 * 16, 0, 16);
#pragma unroll
      for (int s = 0; s < 8; ++s) if (miss & (1u << s)) { if (__all(!act || (v[s][2] == (v[s][0] ^ T1) && v[s][3] == (v[s][1] ^ T2)))) miss &= ~(1u << s); }
      ++npoll;
      if (!miss) break;
      if ((long long)(__builtin_amdgcn_s_memrealtime() - t0) > SPIN_CAP) break;
      const int nm = __builtin_popcount(miss);
      if (nm > 4) __builtin_amdgcn_s_sleep(PSLEEP); else if (nm > 1) __builtin_amdgcn_s_sleep(PSLEEP / 4); else __builtin_amdgcn_s_sleep(1);
    }
    TS(26);
    float s0 = 0.f, s1 = 0.f;
#pragma unroll
    for (int s = 0; s < 8; ++s) { s0 += __uint_as_float(v[s][0]); s1 += __uint_as_float(v[s][1]); }
    const float o0 = dppf<0xB1>(s0), o1 = dppf<0xB1>(s1);
    if (act && !(t & 1)) {
      const int q = idx >> 4, half = (idx >> 3) & 1, n = idx & 7;
      bf16x2 r; r[0] = (__bf16)(s0 + o0); r[1] = (__bf16)(s1 + o1);
      *(bf16x2*)(out + ((size_t)((2 * bp + (n >> 2)) * SQ + (n & 3)) * DM + 36 * kvh + 4 * q + 2 * half) * 2) = r;
    }
    if (kvh == 0 && t == 0) __hip_atomic_store((unsigned*)(ws + WS_EO + bp * 128), eo + 1, __ATOMIC_RELAXED, __HIP_MEMORY_SCOPE_AGENT);
  }
}

__global__ __launch_bounds__(512, 2) void attn_kernel(const char* __restrict__ x, const char* __restrict__ Kc, const char* __restrict__ Vc,
                                                       const char* __restrict__ wq, const char* __restrict__ wo, char* __restrict__ out, char* __restrict__ ws) {
  extern __shared__ __attribute__((aligned(16))) char smem[];
  LAS char* const L = (LAS char*)smem;
  const unsigned lb = (unsigned)(uintptr_t)L;
  const int lane = threadIdx.x & 63, wave = __builtin_amdgcn_readfirstlane(threadIdx.x >> 6);
  const int bid = blockIdx.x;
  const int kvh = (bid & 7) * 2 + ((bid >> 3) & 1), bp = bid >> 4;
  const unsigned a_full = lb + L_FLG, a_done = lb + L_FLG + 16, a_bar = lb + L_FLG + 32, a_lf = lb + L_FLG + 48, a_iss = lb + L_FLG + 80, a_park = lb + L_FLG + 112;
  const int n16 = lane & 15, g = lane >> 4;
  const unsigned tstart = (unsigned)__builtin_amdgcn_s_memrealtime();
  TS_INIT();
  TS(0);
  if (wave == 0) { if (lane < 32) lds_st(lb + L_FLG + lane * 4, 0); lds_drain(); }
  __builtin_amdgcn_s_barrier();
  const unsigned vrow = 4 * g + (n16 >> 2);
  const unsigned vad0 = vrow * 256 + ((vrow & 7) << 5) + (((lane & 3) >> 1) << 4) + ((lane & 1) << 3);
  const char* wo_h = wo + (size_t)kvh * 256 * (DM * 2);
  if (wave == 0) {
    __builtin_amdgcn_s_setprio(3);
    const unsigned r4 = lane >> 4, p = lane & 15;
    unsigned ko[4], vo[2];
#pragma unroll
    for (int i = 0; i < 4; ++i) ko[i] = r4 * 256 + ((p ^ ((4 * i + r4) & 15)) << 4);
#pragma unroll
    for (int i = 0; i < 2; ++i) vo[i] = r4 * 256 + ((p ^ (((4 * i + r4) & 7) << 1)) << 4);
    const size_t pair0 = (size_t)((2 * bp) * HKV + kvh) * (SK * DH * 2), pstep = (size_t)HKV * (SK * DH * 2);
    for (;;) { const u32x4 a = lds_ld4(a_iss), b = lds_ld4(a_iss + 16); if (a[0] && a[1] && a[2] && a[3] && b[0] && b[1] && b[2]) break; }
    TS(2);
    int freeto = N_FREE0;
    int n = 0, pub = 0; unsigned dst = lb, ring_end = lb + NSLOT_A * TILE; int lap = NSLOT_A;
    auto refresh = [&]() {
      bool first = true;
      for (;;) {
        const u32x4 d = lds_ld4(a_done), b = lds_ld4(a_bar);
        int fr = 4 * d[0]; fr = min(fr, (int)(4 * d[1] + 1)); fr = min(fr, (int)(4 * d[2] + 2)); fr = min(fr, (int)(4 * d[3] + 3));
        freeto = all_ge(b, 1) ? fr + lap : N_FREE0;
        if (n < freeto) break;
        if (first) { wait_vm<0>(); pub = n; lds_st(a_full, n); first = false; TS(4); }
        __builtin_amdgcn_s_sleep(1);
      }
    };
    auto publish_landed = [&]() { if (pub < n - 1) { wait_vm<8>(); pub = n - 1; lds_st(a_full, pub); } else __builtin_amdgcn_s_sleep(1); };
#pragma unroll 1
    for (int seg = 0; seg < 4; ++seg) {
      const char* src = ((seg & 1) ? Vc : Kc) + pair0 + (seg >> 1) * pstep;
      const unsigned o0 = (seg & 1) ? vo[0] : ko[0], o1 = (seg & 1) ? vo[1] : ko[1], o2 = (seg & 1) ? vo[0] : ko[2], o3 = (seg & 1) ? vo[1] : ko[3];
      if (seg == 2) { dst = lb; ring_end = lb + NSLOT_B * TILE; freeto -= NSLOT_A - NSLOT_B; lap = NSLOT_B; }
#pragma unroll 1
      for (int t = 0; t < 64; ++t) {
        if (n >= freeto) refresh();
        { const unsigned due = tstart + TORG + ((unsigned)(n * TAU16) >> 4);
          while ((int)((unsigned)__builtin_amdgcn_s_memrealtime() - due) < 0) { publish_landed(); } }
        wait_vm<8 * (INFL - 1)>();
        dma8(o0, o1, o2, o3, src, src + 4096, dst);
        if (n + 1 - INFL > pub) { pub = n + 1 - INFL; lds_st(a_full, pub); }
        src += TILE; dst += TILE; if (dst == ring_end) dst = lb;
        ++n;
      }
    }
    while (!(lds_ld(a_park) && lds_ld(a_park + 4) && lds_ld(a_park + 8))) {}
    bf16x8 WF[8][6];
    auto rdpark = [&](int k0, int k1) {
#pragma unroll
      for (int ks = k0; ks < k1; ++ks)
#pragma unroll
        for (int mt = 0; mt < 6; ++mt) {
          LAS char* tb = L + L_PARK + ks * TILE + (vad0 ^ (mt << 5));
          WF[ks][mt] = CAT8(TRREAD(tb), TRREAD(tb + 4096));
        }
    };
    static_assert(INFL == 3 || INFL == 4, "drain ladder");
    if (INFL == 4) { wait_vm<24>(); if (pub < NTILE - 3) lds_st(a_full, NTILE - 3); }
    rdpark(0, 3); lds_drain(); wait_vm<16>(); if (pub < NTILE - 2) lds_st(a_full, NTILE - 2);
    rdpark(3, 6); lds_drain(); wait_vm<8>(); lds_st(a_full, NTILE - 1);
    wait_vm<0>(); lds_st(a_full, NTILE);
    TS(1);
    rdpark(6, 8);
    while (!all_ge(lds_ld4(a_bar), 6)) {}
    TS(3);
    out_stage<6>(L, lb, WF, 288, wave, lane, bid, kvh, bp, ws, out);
    TS(6);
    TS_DUMP();
    return;
  }
  if (wave < 4) {
    const int h = wave - 1;
    {
      unsigned wo2[2];
#pragma unroll
      for (int i = 0; i < 2; ++i) { const unsigned ri = lane >> 3, f = ((ri >> 1) & 1) | (i << 1); wo2[i] = ri * (NQ * 2) + (((lane & 7) ^ (f << 1)) << 4); }
      const char* wb = wq + (size_t)kvh * 512 + (bp & 3) * 128 + (size_t)(192 * h) * (NQ * 2);
      const unsigned dst0 = lb + L_W + h * 24 * 1024;
      int ii = 3 * (bp >> 2);
#pragma unroll 1
      for (int i = 0; i < 12; ++i) {
        const char* w1 = wb + (size_t)ii * (16 * NQ * 2); const unsigned dst = dst0 + ii * 2048;
        dma1(wo2[0], w1, dst); dma1(wo2[1], w1 + 8 * NQ * 2, dst + 1024);
        ++ii; if (ii == 12) ii = 0;
      }
      lds_st(a_iss + h * 4, 1);
      if (h == 0) {
        const unsigned eq = read_epoch(ws + WS_EQ + (kvh * 4 + (bp >> 2)) * 128), eo = read_epoch(ws + WS_EO + bp * 128);
        lds_st(a_full + 4, eq); lds_st(a_full + 8, eo);
      }
      TS(1);
      wait_vm<0>(); lds_st(a_lf + h * 4, 1);
      TS(3);
    }
    for (;;) { const u32x4 d = lds_ld4(a_done); if (all_ge(d, 30)) break; __builtin_amdgcn_s_sleep(32); }
    TS(4);
    unsigned wv[2];
    { const unsigned r4 = lane >> 4, p = lane & 15;
#pragma unroll
      for (int i = 0; i < 2; ++i) wv[i] = r4 * (DM * 2) + ((p ^ (((4 * i + r4) & 7) << 1)) << 4); }
    auto ld_tile = [&](const char* src, unsigned dst) {
#pragma unroll 1
      for (int i = 0; i < 8; i += 2) {
        dma1(wv[0], src + (size_t)(4 * i) * (DM * 2), dst + i * 1024); dma1(wv[1], src + (size_t)(4 * i + 4) * (DM * 2), dst + (i + 1) * 1024);
        if (i & 2) wait_vm<0>();
      }
    };
    bf16x8 WF[8][6];
    const unsigned stg = L_PARK + h * TILE;
#pragma unroll
    for (int ks = 0; ks < 8; ++ks) {
      ld_tile(wo_h + (size_t)(32 * ks) * (DM * 2) + 192 * h, lb + stg);
#pragma unroll
      for (int mt = 0; mt < 6; ++mt) { LAS char* tb = L + stg + (vad0 ^ (mt << 5)); WF[ks][mt] = CAT8(TRREAD(tb), TRREAD(tb + 4096)); }
      lds_drain();
    }
#pragma unroll 1
    for (int ks = h; ks < 8; ks += 3) ld_tile(wo_h + (size_t)(32 * ks) * (DM * 2) + 576, lb + L_PARK + ks * TILE);
    lds_st(a_park + h * 4, 1);
    TS(5);
    while (!all_ge(lds_ld4(a_bar), 6)) __builtin_amdgcn_s_sleep(1);
    TS(6);
    out_stage<6>(L, lb, WF, 96 * h, wave, lane, bid, kvh, bp, ws, out);
    TS(7);
    TS_DUMP(); return;
  }
  const int c = wave - 4;
  unsigned barv = 0;
  auto cbar = [&]() {
    ++barv; lds_drain();
    lds_st(a_bar + c * 4, barv);
    while (!all_ge(lds_ld4(a_bar), barv)) {}
  };
  unsigned full = 0, done = 0;
  auto wait_full = [&](unsigned m) { while (full <= m) { full = lds_ld(a_full); if (full <= m) __builtin_amdgcn_s_sleep(1); } };
  {
    const int rg = bp >> 2, cb = bp & 3, nt = c & 1, ch = c >> 1;
    {
      const char* xb = x + (size_t)(rg * 32) * (DM * 2);
      unsigned dst = lb + L_X + c * 9 * 1024;
      unsigned P = c * 9 * 64 + lane;
#pragma unroll 1
      for (int i = 0; i < 9; ++i) {
        const unsigned n = (P * 58255u) >> 22;
        dma1((P ^ ((n >> 1) & 7)) << 4, xb, dst);
        P += 64; dst += 1024;
      }
      lds_st(a_iss + 12 + c * 4, 1);
    }
    TS(9);
    wait_vm<0>(); lds_st(a_lf + 12 + c * 4, 1);
    TS(10);
    for (;;) { const u32x4 a = lds_ld4(a_lf), b = lds_ld4(a_lf + 16); if (a[0] && a[1] && a[2] && a[3] && b[0] && b[1] && b[2]) break; }
    TS(13);
    const unsigned eq = lds_ld(a_full + 4);
    const unsigned T1 = 2 * eq + 3, T2 = T1 * 0x9E3779B1u;
    const int q4 = n16 >> 2, pp = lane & 3;
    unsigned wad[2][2];
#pragma unroll
    for (int t = 0; t < 2; ++t) { const int row = 8 * g + 4 * t + q4, f = ((row >> 1) & 1) | ((g & 1) << 1);
#pragma unroll
      for (int u = 0; u < 2; ++u) wad[t][u] = L_W + row * 128 + ((f ^ (2 * ch + u)) << 5) + ((pp >> 1) << 4) + ((pp & 1) << 3); }
    f32x4 acc[2] = {};
    const int xn = nt * 16 + n16, xs = (xn >> 1) & 7;
    LAS char* xr0 = L + L_X + (xn * 72 + (g ^ xs)) * 16;
    LAS char* xr1 = L + L_X + (xn * 72 + ((4 + g) ^ xs)) * 16;
    bf16x8 XB[18], WA[2][3][2];
#pragma unroll
    for (int ks = 0; ks < 18; ++ks) XB[ks] = *(LAS bf16x8*)(((ks & 1) ? xr1 : xr0) + (ks >> 1) * 128);
    auto rdw = [&](int grp) {
#pragma unroll
      for (int k = 0; k < 3; ++k)
#pragma unroll
        for (int u = 0; u < 2; ++u)
          WA[grp & 1][k][u] = CAT8(TRREAD(L + wad[0][u] + (grp * 3 + k) * 4096), TRREAD(L + wad[1][u] + (grp * 3 + k) * 4096));
    };
    rdw(0);
#pragma unroll
    for (int grp = 0; grp < 6; ++grp) {
      __builtin_amdgcn_sched_barrier(0);
      if (grp + 1 < 6) rdw(grp + 1);
      __builtin_amdgcn_sched_barrier(0);
#pragma unroll
      for (int k = 0; k < 3; ++k)
#pragma unroll
        for (int u = 0; u < 2; ++u) acc[u] = MFMA(WA[grp & 1][k][u], XB[grp * 3 + k], acc[u]);
    }
    __builtin_amdgcn_sched_barrier(0);
    ++barv; lds_drain(); lds_st(a_bar + c * 4, barv);
    TS(11);
    const __amdgpu_buffer_rsrc_t rq = __builtin_amdgcn_make_buffer_rsrc(ws + WS_QM, 0, QM_BYTES, 0x00020000);
    {
      const int dbp = rg * 4 + nt * 2 + (n16 >> 3), dbid = (dbp << 4) | (bid & 15);
#pragma unroll
      for (int u = 0; u < 2; ++u) {
        const f32x4 v = acc[u];
        bf16x4 h; h[0] = (__bf16)v[0]; h[1] = (__bf16)v[1]; h[2] = (__bf16)v[2]; h[3] = (__bf16)v[3];
        const u32x2 w = __builtin_bit_cast(u32x2, h);
        const u32x4 chk = {w[0], w[1], w[0] ^ T1, w[1] ^ T2};
        __builtin_amdgcn_raw_buffer_store_b128(chk, rq, dbid * 8192 + (cb * 128 + (2 * ch + u) * 32 + (n16 & 7) * 4 + g) * 16, 0, 16);
      }
    }
    TS(12);
    {
      __builtin_amdgcn_s_sleep(PDELAY);
      u32x4 v0, v1; const unsigned off = bid * 8192 + (c * 128 + lane) * 16;
      const unsigned long long t0 = __builtin_amdgcn_s_memrealtime();
      for (;;) {
        asm volatile("" ::: "memory");
        v0 = __builtin_amdgcn_raw_buffer_load_b128(rq, off, 0, 16);
        v1 = __builtin_amdgcn_raw_buffer_load_b128(rq, off + 1024, 0, 16);
        const bool ok = v0[2] == (v0[0] ^ T1) && v0[3] == (v0[1] ^ T2) && v1[2] == (v1[0] ^ T1) && v1[3] == (v1[1] ^ T2);
        if (__all(ok)) break;
        if ((long long)(__builtin_amdgcn_s_memrealtime() - t0) > SPIN_CAP) break;
        __builtin_amdgcn_s_sleep(2);
      }
      TS(14);
      const int row = (lane >> 2) & 7, gg = lane & 3, r = c >> 1;
      LAS char* qd = L + L_QO + ((row >> 2) * 8 + r * 4 + (row & 3)) * QO_PITCH + (64 * (c & 1) + 16 * (lane >> 5) + 4 * gg) * 2;
      *(LAS u32x2*)(qd) = u32x2{v0[0], v0[1]};
      *(LAS u32x2*)(qd + 64) = u32x2{v1[0], v1[1]};
    }
    cbar();
    if (cb == 0 && c == 0 && lane == 0) __hip_atomic_store((unsigned*)(ws + WS_EQ + (kvh * 4 + rg) * 128), eq + 1, __ATOMIC_RELAXED, __HIP_MEMORY_SCOPE_AGENT);
  }
  TS(2);
  unsigned kad[4];
#pragma unroll
  for (int s = 0; s < 4; ++s) kad[s] = n16 * 256 + (((4 * s + g) ^ n16) << 4);
  const unsigned mlo = n16 < 8 ? 0xffffffffu : 0u;
  int slot = c;
  int mi = c;
  auto next_tile = [&]() -> LAS char* {
    wait_full(mi);
    LAS char* tb = L + slot * TILE;
    const int mn = mi + 4;
    if (mn < N_SW) { slot += 4; if (slot >= NSLOT_A) slot -= NSLOT_A; }
    else if (mi < N_SW) slot = mn - N_SW;
    else { slot += 4; if (slot >= NSLOT_B) slot -= NSLOT_B; }
    mi = mn;
    return tb;
  };
  bf16x8 WF[8][3];
  const int wsb = min(768 + 96 * c, 1024);
  const unsigned wsof = (lane >> 3) * (DM * 2) + (lane & 7) * 16;
  const unsigned wrd = L_SCR + c * SCR_W + vrow * 128 + (768 + 96 * c - wsb) + (((lane & 3) >> 1) << 4) + ((lane & 1) << 3);
  auto wo_issue = [&](int ks) {
    const char* src = wo_h + (size_t)(32 * ks) * (DM * 2) + wsb;
#pragma unroll
    for (int i = 0; i < 4; ++i) dma1(wsof, src + (size_t)(8 * i) * (DM * 2), lb + L_SCR + c * SCR_W + i * 1024);
  };
  auto wo_read = [&](int ks) {
    wait_vm<0>();
#pragma unroll
    for (int mt = 0; mt < 3; ++mt) WF[ks][mt] = CAT8(TRREAD(L + wrd + mt * 32), TRREAD(L + wrd + mt * 32 + 2048));
    lds_drain();
  };
  auto pair = [&](auto PC) {
    constexpr int p = decltype(PC)::value;
    bf16x8 QA[4], QB[4];
#pragma unroll
    for (int s = 0; s < 4; ++s) {
      const u32x4 v = *(LAS u32x4*)(L + L_QO + (p * 8 + (n16 & 7)) * QO_PITCH + (4 * s + g) * 16);
      QA[s] = __builtin_bit_cast(bf16x8, v & mlo); QB[s] = __builtin_bit_cast(bf16x8, v & ~mlo);
    }
    f32x4 S[8][2];
#pragma unroll
    for (int i = 0; i < 8; ++i) {
      S[i][0] = f32x4{0.f, 0.f, 0.f, 0.f}; S[i][1] = S[i][0];
#pragma unroll
      for (int h = 0; h < 2; ++h) {
        LAS char* tb = next_tile();
        bf16x8 kf[2][4];
#pragma unroll
        for (int rb = 0; rb < 2; ++rb)
#pragma unroll
          for (int s = 0; s < 4; ++s) kf[rb][s] = *(LAS bf16x8*)(tb + kad[s] + rb * 4096);
        ++done; lds_st(a_done + c * 4, done);
#pragma unroll
        for (int s = 0; s < 4; ++s)
#pragma unroll
          for (int rb = 0; rb < 2; ++rb) S[i][rb] = MFMA(kf[rb][s], h ? QB[s] : QA[s], S[i][rb]);
      }
    }
    if (p == 0) TS(3); else TS(6);
    float m = S[0][0][0];
#pragma unroll
    for (int i = 0; i < 8; ++i)
#pragma unroll
      for (int rb = 0; rb < 2; ++rb)
#pragma unroll
        for (int j = 0; j < 4; ++j) m = fmaxf(m, S[i][rb][j]);
    m = fmaxf(m, dppf<0x128>(m));
    m = xrow_max(m);
    const float mc = m * C2;
    float l = 0.f;
    u32x4 P[8];
#pragma unroll
    for (int i = 0; i < 8; ++i) {
      bf16x8 pk;
#pragma unroll
      for (int rb = 0; rb < 2; ++rb)
#pragma unroll
        for (int j = 0; j < 4; ++j) { const float e = __builtin_amdgcn_exp2f(fmaf(S[i][rb][j], C2, -mc)); l += e; pk[rb * 4 + j] = (__bf16)e; }
      P[i] = __builtin_bit_cast(u32x4, pk);
    }
    l += dppf<0x128>(l);
    l = xrow_sum(l);
    if (lane < 8) { *(LAS float*)(L + L_ML + (c * 8 + lane) * 8) = mc; *(LAS float*)(L + L_ML + (c * 8 + lane) * 8 + 4) = l; }
    f32x4 O[8];
#pragma unroll
    for (int cb = 0; cb < 8; ++cb) O[cb] = f32x4{0.f, 0.f, 0.f, 0.f};
#pragma unroll
    for (int i = 0; i < 8; ++i) {
      if (p == 1) { if (i > 0) wo_read(i - 1); wo_issue(i); }
#pragma unroll
      for (int h = 0; h < 2; ++h) {
        const bf16x8 pb = __builtin_bit_cast(bf16x8, P[i] & (h ? ~mlo : mlo));
        if (p == 1 && i == 7 && h == 1) wo_read(7);
        LAS char* tb = next_tile();
        bf16x8 vf[8];
#pragma unroll
        for (int cb = 0; cb < 8; ++cb) vf[cb] = CAT8(TRREAD(tb + (vad0 ^ (cb << 5))), TRREAD(tb + (vad0 ^ (cb << 5)) + 4096));
        ++done; lds_st(a_done + c * 4, done);
#pragma unroll
        for (int cb = 0; cb < 8; ++cb) O[cb] = MFMA(vf[cb], pb, O[cb]);
      }
    }
    if (p == 0) TS(4); else TS(7);
#pragma unroll
    for (int cb = 0; cb < 8; ++cb)
#pragma unroll
      for (int j = 0; j < 4; ++j) O[cb][j] += dppf<0x128>(O[cb][j]);
    {
      LAS char* dst = L + L_SCR + c * SCR_W + (n16 & 7) * SCR_PITCH + g * 16 + (n16 >> 3) * 256;
#pragma unroll
      for (int cb = 0; cb < 4; ++cb) *(LAS f32x4*)(dst + cb * 64) = (n16 >> 3) ? O[4 + cb] : O[cb];
    }
    cbar();
    {
      const int tid = c * 64 + lane, q = tid >> 5, d4 = (tid & 31) * 4;
      float mw[4], lw[4];
#pragma unroll
      for (int w = 0; w < 4; ++w) { mw[w] = *(LAS float*)(L + L_ML + (w * 8 + q) * 8); lw[w] = *(LAS float*)(L + L_ML + (w * 8 + q) * 8 + 4); }
      const float M = fmaxf(fmaxf(mw[0], mw[1]), fmaxf(mw[2], mw[3]));
      float Ls = 0.f; f32x4 o = {0.f, 0.f, 0.f, 0.f};
#pragma unroll
      for (int w = 0; w < 4; ++w) { const float f = __builtin_amdgcn_exp2f(mw[w] - M); Ls += f * lw[w];
        const f32x4 v = *(LAS f32x4*)(L + L_SCR + w * SCR_W + q * SCR_PITCH + d4 * 4); o += v * f; }
      const float inv = 1.f / Ls;
      bf16x4 ob; ob[0] = (__bf16)(o[0] * inv); ob[1] = (__bf16)(o[1] * inv); ob[2] = (__bf16)(o[2] * inv); ob[3] = (__bf16)(o[3] * inv);
      *(LAS bf16x4*)(L + L_QO + (p * 8 + q) * QO_PITCH + d4 * 2) = ob;
    }
    cbar();
    if (p == 0) TS(5); else TS(8);
  };
  pair(std::integral_constant<int, 0>{});
  pair(std::integral_constant<int, 1>{});
  out_stage<3>(L, lb, WF, 384 + 48 * c, wave, lane, bid, kvh, bp, ws, out);
  TS(15);
  TS_DUMP();
}
}

extern "C" void kernel_launch(void* const* d_in, const int* in_sizes, int n_in, void* d_out, int out_size, void* d_ws, size_t ws_size, hipStream_t stream) {
  using namespace gqa;
  (void)hipFuncSetAttribute((const void*)attn_kernel, hipFuncAttributeMaxDynamicSharedMemorySize, LDS_TOTAL);
  hipLaunchKernelGGL(attn_kernel, dim3(256), dim3(512), LDS_TOTAL, stream, (const char*)d_in[0], (const char*)d_in[1], (const char*)d_in[2], (const char*)d_in[3],
                     (const char*)d_in[4], (char*)d_out, (char*)d_ws);
}
```

```cpp
#include <hip/hip_runtime.h>
#include <stdint.h>
#include <type_traits>
#include <stdio.h>

namespace gqa {
typedef __bf16 bf16x8 __attribute__((ext_vector_type(8)));
typedef __bf16 bf16x4 __attribute__((ext_vector_type(4)));
typedef __bf16 bf16x2 __attribute__((ext_vector_type(2)));
typedef float f32x4 __attribute__((ext_vector_type(4)));
typedef unsigned u32x4 __attribute__((ext_vector_type(4)));
typedef unsigned u32x2 __attribute__((ext_vector_type(2)));
#define LAS __attribute__((address_space(3)))

constexpr int SQ = 4, DM = 576, HKV = 16, SK = 2048, DH = 128, NQ = 4096;
#define INFL 4
#define KDELAY -1
#define PSLEEP 32
#define PDELAY 12
#define TAU16 440
#define TORG 330
constexpr int TILE = 8192, NTILE = 256;
constexpr int NSLOT_A = 17, NSLOT_B = 9, N_SW = 128;
constexpr unsigned L_W = 4 * TILE, L_X = 13 * TILE;
constexpr unsigned L_PARK = NSLOT_B * TILE;
constexpr int N_FREE0 = 4;
constexpr unsigned L_SCR = NSLOT_A * TILE, SCR_PITCH = 528, SCR_W = 8 * SCR_PITCH;
constexpr unsigned QO_PITCH = 272;
constexpr unsigned L_QO = L_SCR + 17408;
constexpr unsigned L_ML = L_QO + 16 * QO_PITCH;
constexpr unsigned L_FLG = L_ML + 256;
constexpr unsigned LDS_BYTES = L_FLG + 128;
constexpr float C2 = 0.08838834764831845f * 1.4426950408889634f;
constexpr size_t WS_EQ = 0, WS_EO = 64 * 128, WS_QM = 65536, WS_OM = 4u << 20;
constexpr unsigned QM_BYTES = 256 * 8192, OM_STRIDE = 2304 * 16, OM_BYTES = 256 * OM_STRIDE;
constexpr long long SPIN_CAP = 2000000;

__device__ __forceinline__ unsigned lds_ld(unsigned addr) {
  unsigned v; asm volatile("ds_read_b32 %0, %1\n\ts_waitcnt lgkmcnt(0)" : "=v"(v) : "v"(addr) : "memory");
  return __builtin_amdgcn_readfirstlane(v);
}
__device__ __forceinline__ u32x4 lds_ld4(unsigned addr) {
  u32x4 v; asm volatile("ds_read_b128 %0, %1\n\ts_waitcnt lgkmcnt(0)" : "=v"(v) : "v"(addr) : "memory");
  u32x4 r; r[0] = __builtin_amdgcn_readfirstlane(v[0]); r[1] = __builtin_amdgcn_readfirstlane(v[1]);
  r[2] = __builtin_amdgcn_readfirstlane(v[2]); r[3] = __builtin_amdgcn_readfirstlane(v[3]); return r;
}
__device__ __forceinline__ void lds_st(unsigned addr, unsigned val) {
  asm volatile("ds_write_b32 %0, %1" :: "v"(addr), "v"(val) : "memory");
}
__device__ __forceinline__ void lds_drain() { asm volatile("s_waitcnt lgkmcnt(0)" ::: "memory"); }
__device__ __forceinline__ void dma8(unsigned o0, unsigned o1, unsigned o2, unsigned o3, const char* src, const char* src2, unsigned lds) {
  asm volatile("s_mov_b32 m0, %6\n\ts_nop 0\n\t"
               "global_load_lds_dwordx4 %0, %4 nt\n\t"
               "global_load_lds_dwordx4 %1, %4 offset:1024 nt\n\t"
               "global_load_lds_dwordx4 %2, %4 offset:2048 nt\n\t"
               "global_load_lds_dwordx4 %3, %4 offset:3072 nt\n\t"
               "s_add_u32 m0, m0, 0x1000\n\ts_nop 0\n\t"
               "global_load_lds_dwordx4 %0, %5 nt\n\t"
               "global_load_lds_dwordx4 %1, %5 offset:1024 nt\n\t"
               "global_load_lds_dwordx4 %2, %5 offset:2048 nt\n\t"
               "global_load_lds_dwordx4 %3, %5 offset:3072 nt"
               :: "v"(o0), "v"(o1), "v"(o2), "v"(o3), "s"(src), "s"(src2), "s"(lds) : "memory", "scc");
}
__device__ __forceinline__ void dma1(unsigned o, const char* src, unsigned lds) {
  asm volatile("s_mov_b32 m0, %2\n\ts_nop 0\n\tglobal_load_lds_dwordx4 %0, %1 nt" :: "v"(o), "s"(src), "s"(lds) : "memory");
}
template <int N> __device__ __forceinline__ void wait_vm() { asm volatile("s_waitcnt vmcnt(%0)" :: "n"(N) : "memory"); }
template <int CTRL> __device__ __forceinline__ float dppf(float x) {
  return __builtin_bit_cast(float, __builtin_amdgcn_mov_dpp(__builtin_bit_cast(int, x), CTRL, 0xf, 0xf, true));
}
__device__ __forceinline__ float xrow_max(float x) {
  auto s = __builtin_amdgcn_permlane16_swap(__float_as_uint(x), __float_as_uint(x), false, false);
  x = fmaxf(__uint_as_float(s[0]), __uint_as_float(s[1]));
  auto t = __builtin_amdgcn_permlane32_swap(__float_as_uint(x), __float_as_uint(x), false, false);
  return fmaxf(__uint_as_float(t[0]), __uint_as_float(t[1]));
}
__device__ __forceinline__ float xrow_sum(float x) {
  auto s = __builtin_amdgcn_permlane16_swap(__float_as_uint(x), __float_as_uint(x), false, false);
  x = __uint_as_float(s[0]) + __uint_as_float(s[1]);
  auto t = __builtin_amdgcn_permlane32_swap(__float_as_uint(x), __float_as_uint(x), false, false);
  return __uint_as_float(t[0]) + __uint_as_float(t[1]);
}
__device__ __forceinline__ unsigned read_epoch(const char* p) {
  unsigned e; asm volatile("s_load_dword %0, %1, 0x0\n\ts_waitcnt lgkmcnt(0)" : "=s"(e) : "s"(p) : "memory"); return e;
}
__device__ __forceinline__ bool all_ge(const u32x4 v, unsigned k) { return v[0] >= k && v[1] >= k && v[2] >= k && v[3] >= k; }
#define MFMA(a, b, c) __builtin_amdgcn_mfma_f32_16x16x32_bf16((a), (b), (c), 0, 0, 0)
#define TRREAD(p) __builtin_amdgcn_ds_read_tr16_b64_v4bf16((LAS bf16x4*)(p))
#define CAT8(lo, hi) __builtin_shufflevector((lo), (hi), 0, 1, 2, 3, 4, 5, 6, 7)

constexpr unsigned LDS_TOTAL = LDS_BYTES;
#define TS(slot) do {} while (0)
#define TS_INIT() do {} while (0)
#define TS_DUMP() do {} while (0)

template <int NM>
__device__ __forceinline__ void out_stage(LAS char* L, unsigned lb, const bf16x8 (&WF)[8][NM], int col0, int wave, int lane, int bid, int kvh, int bp,
                                          char* __restrict__ ws, char* __restrict__ out) {
  const int n16 = lane & 15, g = lane >> 4;
  const unsigned eo = lds_ld(lb + L_FLG + 8);
  const unsigned T1 = 2 * eo + 3, T2 = T1 * 0x9E3779B1u;
  f32x4 acc[NM];
#pragma unroll
  for (int mt = 0; mt < NM; ++mt) acc[mt] = f32x4{0.f, 0.f, 0.f, 0.f};
  LAS char* ob = L + L_QO + ((n16 >> 2) & 1) * 8 * QO_PITCH + (n16 & 3) * QO_PITCH + g * 8;
  bf16x8 B[8];
#pragma unroll
  for (int ks = 0; ks < 8; ++ks) {
    LAS char* a = ob + (ks >> 2) * 4 * QO_PITCH + (ks & 3) * 64;
    B[ks] = CAT8(*(LAS bf16x4*)a, *(LAS bf16x4*)(a + 32));
  }
#pragma unroll
  for (int ks = 0; ks < 8; ++ks)
#pragma unroll
    for (int mt = 0; mt < NM; ++mt) acc[mt] = MFMA(WF[ks][mt], B[ks], acc[mt]);
  asm volatile("" :: "v"(acc[0]), "v"(acc[NM - 1]));
  TS(24);
  const __amdgpu_buffer_rsrc_t ro = __builtin_amdgcn_make_buffer_rsrc(ws + WS_OM, 0, OM_BYTES, 0x00020000);
  if (n16 < 8) {
#pragma unroll
    for (int mt = 0; mt < NM; ++mt) {
      const int cc = col0 + 16 * mt + 4 * g, j = cc / 36, q = (cc - 36 * j) >> 2;
      const int dbid = (bp << 4) | (j >> 1) | ((j & 1) << 3);
      const unsigned off = dbid * OM_STRIDE + (((kvh * 9 + q) * 2) * 8 + n16) * 16;
      const u32x4 a = __builtin_bit_cast(u32x4, acc[mt]);
      __builtin_amdgcn_raw_buffer_store_b128(u32x4{a[0], a[1], a[0] ^ T1, a[1] ^ T2}, ro, off, 0, 16);
      __builtin_amdgcn_raw_buffer_store_b128(u32x4{a[2], a[3], a[2] ^ T1, a[3] ^ T2}, ro, off + 128, 0, 16);
    }
  }
  TS(25);
  const int t = wave * 64 + lane;
  if (wave < 5) {
    int npoll = 0;
    const bool act = t < 288;
    const int idx = act ? (t >> 1) : 0;
    const unsigned off = bid * OM_STRIDE + ((t & 1) * 8 * 144 + idx) * 16;
    u32x4 v[8];
    unsigned miss = 0xff;
    const unsigned long long t0 = __builtin_amdgcn_s_memrealtime();
    for (;;) {
      asm volatile("" ::: "memory");
#pragma unroll
      for (int s = 0; s < 8; ++s) if (miss & (1u << s)) v[s] = __builtin_amdgcn_raw_buffer_load_b128(ro, off + s * 144 * 16, 0, 16);
#pragma unroll
      for (int s = 0; s < 8; ++s) if (miss & (1u << s)) { if (__all(!act || (v[s][2] == (v[s][0] ^ T1) && v[s][3] == (v[s][1] ^ T2)))) miss &= ~(1u << s); }
      ++npoll;
      if (!miss) break;
      if ((long long)(__builtin_amdgcn_s_memrealtime() - t0) > SPIN_CAP) break;
      const int nm = __builtin_popcount(miss);
      if (nm > 4) __builtin_amdgcn_s_sleep(PSLEEP); else if (nm > 1) __builtin_amdgcn_s_sleep(PSLEEP / 4); else __builtin_amdgcn_s_sleep(1);
    }
    TS(26);
    float s0 = 0.f, s1 = 0.f;
#pragma unroll
    for (int s = 0; s < 8; ++s) { s0 += __uint_as_float(v[s][0]); s1 += __uint_as_float(v[s][1]); }
    const float o0 = dppf<0xB1>(s0), o1 = dppf<0xB1>(s1);
    if (act && !(t & 1)) {
      const int q = idx >> 4, half = (idx >> 3) & 1, n = idx & 7;
      bf16x2 r; r[0] = (__bf16)(s0 + o0); r[1] = (__bf16)(s1 + o1);
      *(bf16x2*)(out + ((size_t)((2 * bp + (n >> 2)) * SQ + (n & 3)) * DM + 36 * kvh + 4 * q + 2 * half) * 2) = r;
    }
    if (kvh == 0 && t == 0) __hip_atomic_store((unsigned*)(ws + WS_EO + bp * 128), eo + 1, __ATOMIC_RELAXED, __HIP_MEMORY_SCOPE_AGENT);
  }
}

__global__ __launch_bounds__(512, 2) void attn_kernel(const char* __restrict__ x, const char* __restrict__ Kc, const char* __restrict__ Vc,
                                                       const char* __restrict__ wq, const char* __restrict__ wo, char* __restrict__ out, char* __restrict__ ws) {
  extern __shared__ __attribute__((aligned(16))) char smem[];
  LAS char* const L = (LAS char*)smem;
  const unsigned lb = (unsigned)(uintptr_t)L;
  const int lane = threadIdx.x & 63, wave = __builtin_amdgcn_readfirstlane(threadIdx.x >> 6);
  const int bid = blockIdx.x;
  const int kvh = (bid & 7) * 2 + ((bid >> 3) & 1), bp = bid >> 4;
  const unsigned a_full = lb + L_FLG, a_done = lb + L_FLG + 16, a_bar = lb + L_FLG + 32, a_lf = lb + L_FLG + 48, a_iss = lb + L_FLG + 80, a_park = lb + L_FLG + 112;
  const int n16 = lane & 15, g = lane >> 4;
  const unsigned tstart = (unsigned)__builtin_amdgcn_s_memrealtime();
  const unsigned long long cstart = __builtin_amdgcn_s_memtime();
  TS_INIT();
  TS(0);
  if (wave == 0) { if (lane < 32) lds_st(lb + L_FLG + lane * 4, 0); lds_drain(); }
  __builtin_amdgcn_s_barrier();
  const unsigned vrow = 4 * g + (n16 >> 2);
  const unsigned vad0 = vrow * 256 + ((vrow & 7) << 5) + (((lane & 3) >> 1) << 4) + ((lane & 1) << 3);
  const char* wo_h = wo + (size_t)kvh * 256 * (DM * 2);
  if (wave == 0) {
    __builtin_amdgcn_s_setprio(3);
    const unsigned r4 = lane >> 4, p = lane & 15;
    unsigned ko[4], vo[2];
#pragma unroll
    for (int i = 0; i < 4; ++i) ko[i] = r4 * 256 + ((p ^ ((4 * i + r4) & 15)) << 4);
#pragma unroll
    for (int i = 0; i < 2; ++i) vo[i] = r4 * 256 + ((p ^ (((4 * i + r4) & 7) << 1)) << 4);
    const size_t pair0 = (size_t)((2 * bp) * HKV + kvh) * (SK * DH * 2), pstep = (size_t)HKV * (SK * DH * 2);
    for (;;) { const u32x4 a = lds_ld4(a_iss), b = lds_ld4(a_iss + 16); if (a[0] && a[1] && a[2] && a[3] && b[0] && b[1] && b[2]) break; }
    TS(2);
    int freeto = N_FREE0;
    int n = 0, pub = 0; unsigned dst = lb, ring_end = lb + NSLOT_A * TILE; int lap = NSLOT_A;
    auto refresh = [&]() {
      bool first = true;
      for (;;) {
        const u32x4 d = lds_ld4(a_done), b = lds_ld4(a_bar);
        int fr = 4 * d[0]; fr = min(fr, (int)(4 * d[1] + 1)); fr = min(fr, (int)(4 * d[2] + 2)); fr = min(fr, (int)(4 * d[3] + 3));
        freeto = all_ge(b, 1) ? fr + lap : N_FREE0;
        if (n < freeto) break;
        if (first) { wait_vm<0>(); pub = n; lds_st(a_full, n); first = false; TS(4); }
        __builtin_amdgcn_s_sleep(1);
      }
    };
    auto publish_landed = [&]() { if (pub < n - 1) { wait_vm<8>(); pub = n - 1; lds_st(a_full, pub); } else __builtin_amdgcn_s_sleep(1); };
#pragma unroll 1
    for (int seg = 0; seg < 4; ++seg) {
      const char* src = ((seg & 1) ? Vc : Kc) + pair0 + (seg >> 1) * pstep;
      const unsigned o0 = (seg & 1) ? vo[0] : ko[0], o1 = (seg & 1) ? vo[1] : ko[1], o2 = (seg & 1) ? vo[0] : ko[2], o3 = (seg & 1) ? vo[1] : ko[3];
      if (seg == 2) { dst = lb; ring_end = lb + NSLOT_B * TILE; freeto -= NSLOT_A - NSLOT_B; lap = NSLOT_B; }
#pragma unroll 1
      for (int t = 0; t < 64; ++t) {
        if (n >= freeto) refresh();
        { const unsigned due = tstart + TORG + ((unsigned)(n * TAU16) >> 4);
          while ((int)((unsigned)__builtin_amdgcn_s_memrealtime() - due) < 0) { publish_landed(); } }
        wait_vm<8 * (INFL - 1)>();
        dma8(o0, o1, o2, o3, src, src + 4096, dst);
        if (n + 1 - INFL > pub) { pub = n + 1 - INFL; lds_st(a_full, pub); }
        src += TILE; dst += TILE; if (dst == ring_end) dst = lb;
        ++n;
      }
    }
    while (!(lds_ld(a_park) && lds_ld(a_park + 4) && lds_ld(a_park + 8))) {}
    bf16x8 WF[8][6];
    auto rdpark = [&](int k0, int k1) {
#pragma unroll
      for (int ks = k0; ks < k1; ++ks)
#pragma unroll
        for (int mt = 0; mt < 6; ++mt) {
          LAS char* tb = L + L_PARK + ks * TILE + (vad0 ^ (mt << 5));
          WF[ks][mt] = CAT8(TRREAD(tb), TRREAD(tb + 4096));
        }
    };
    static_assert(INFL == 3 || INFL == 4, "drain ladder");
    if (INFL == 4) { wait_vm<24>(); if (pub < NTILE - 3) lds_st(a_full, NTILE - 3); }
    rdpark(0, 3); lds_drain(); wait_vm<16>(); if (pub < NTILE - 2) lds_st(a_full, NTILE - 2);
    rdpark(3, 6); lds_drain(); wait_vm<8>(); lds_st(a_full, NTILE - 1);
    wait_vm<0>(); lds_st(a_full, NTILE);
    TS(1);
    rdpark(6, 8);
    while (!all_ge(lds_ld4(a_bar), 6)) {}
    TS(3);
    out_stage<6>(L, lb, WF, 288, wave, lane, bid, kvh, bp, ws, out);
    TS(6);
    TS_DUMP();
    return;
  }
  if (wave < 4) {
    const int h = wave - 1;
    {
      unsigned wo2[2];
#pragma unroll
      for (int i = 0; i < 2; ++i) { const unsigned ri = lane >> 3, f = ((ri >> 1) & 1) | (i << 1); wo2[i] = ri * (NQ * 2) + (((lane & 7) ^ (f << 1)) << 4); }
      const char* wb = wq + (size_t)kvh * 512 + (bp & 3) * 128 + (size_t)(192 * h) * (NQ * 2);
      const unsigned dst0 = lb + L_W + h * 24 * 1024;
      int ii = 3 * (bp >> 2);
#pragma unroll 1
      for (int i = 0; i < 12; ++i) {
        const char* w1 = wb + (size_t)ii * (16 * NQ * 2); const unsigned dst = dst0 + ii * 2048;
        dma1(wo2[0], w1, dst); dma1(wo2[1], w1 + 8 * NQ * 2, dst + 1024);
        ++ii; if (ii == 12) ii = 0;
      }
      lds_st(a_iss + h * 4, 1);
      if (h == 0) {
        const unsigned eq = read_epoch(ws + WS_EQ + (kvh * 4 + (bp >> 2)) * 128), eo = read_epoch(ws + WS_EO + bp * 128);
        lds_st(a_full + 4, eq); lds_st(a_full + 8, eo);
      }
      TS(1);
      wait_vm<0>(); lds_st(a_lf + h * 4, 1);
      TS(3);
    }
    for (;;) { const u32x4 d = lds_ld4(a_done); if (all_ge(d, 30)) break; __builtin_amdgcn_s_sleep(32); }
    TS(4);
    unsigned wv[2];
    { const unsigned r4 = lane >> 4, p = lane & 15;
#pragma unroll
      for (int i = 0; i < 2; ++i) wv[i] = r4 * (DM * 2) + ((p ^ (((4 * i + r4) & 7) << 1)) << 4); }
    auto ld_tile = [&](const char* src, unsigned dst) {
#pragma unroll 1
      for (int i = 0; i < 8; i += 2) {
        dma1(wv[0], src + (size_t)(4 * i) * (DM * 2), dst + i * 1024); dma1(wv[1], src + (size_t)(4 * i + 4) * (DM * 2), dst + (i + 1) * 1024);
        if (i & 2) wait_vm<0>();
      }
    };
    bf16x8 WF[8][6];
    const unsigned stg = L_PARK + h * TILE;
#pragma unroll
    for (int ks = 0; ks < 8; ++ks) {
      ld_tile(wo_h + (size_t)(32 * ks) * (DM * 2) + 192 * h, lb + stg);
#pragma unroll
      for (int mt = 0; mt < 6; ++mt) { LAS char* tb = L + stg + (vad0 ^ (mt << 5)); WF[ks][mt] = CAT8(TRREAD(tb), TRREAD(tb + 4096)); }
      lds_drain();
    }
#pragma unroll 1
    for (int ks = h; ks < 8; ks += 3) ld_tile(wo_h + (size_t)(32 * ks) * (DM * 2) + 576, lb + L_PARK + ks * TILE);
    lds_st(a_park + h * 4, 1);
    TS(5);
    while (!all_ge(lds_ld4(a_bar), 6)) __builtin_amdgcn_s_sleep(1);
    TS(6);
    out_stage<6>(L, lb, WF, 96 * h, wave, lane, bid, kvh, bp, ws, out);
    TS(7);
    TS_DUMP(); return;
  }
  const int c = wave - 4;
  unsigned barv = 0;
  auto cbar = [&]() {
    ++barv; lds_drain();
    lds_st(a_bar + c * 4, barv);
    while (!all_ge(lds_ld4(a_bar), barv)) {}
  };
  unsigned full = 0, done = 0;
  auto wait_full = [&](unsigned m) { while (full <= m) { full = lds_ld(a_full); if (full <= m) __builtin_amdgcn_s_sleep(1); } };
  {
    const int rg = bp >> 2, cb = bp & 3, nt = c & 1, ch = c >> 1;
    {
      const char* xb = x + (size_t)(rg * 32) * (DM * 2);
      unsigned dst = lb + L_X + c * 9 * 1024;
      unsigned P = c * 9 * 64 + lane;
#pragma unroll 1
      for (int i = 0; i < 9; ++i) {
        const unsigned n = (P * 58255u) >> 22;
        dma1((P ^ ((n >> 1) & 7)) << 4, xb, dst);
        P += 64; dst += 1024;
      }
      lds_st(a_iss + 12 + c * 4, 1);
    }
    TS(9);
    wait_vm<0>(); lds_st(a_lf + 12 + c * 4, 1);
    TS(10);
    for (;;) { const u32x4 a = lds_ld4(a_lf), b = lds_ld4(a_lf + 16); if (a[0] && a[1] && a[2] && a[3] && b[0] && b[1] && b[2]) break; }
    TS(13);
    const unsigned eq = lds_ld(a_full + 4);
    const unsigned T1 = 2 * eq + 3, T2 = T1 * 0x9E3779B1u;
    const int q4 = n16 >> 2, pp = lane & 3;
    unsigned wad[2][2];
#pragma unroll
    for (int t = 0; t < 2; ++t) { const int row = 8 * g + 4 * t + q4, f = ((row >> 1) & 1) | ((g & 1) << 1);
#pragma unroll
      for (int u = 0; u < 2; ++u) wad[t][u] = L_W + row * 128 + ((f ^ (2 * ch + u)) << 5) + ((pp >> 1) << 4) + ((pp & 1) << 3); }
    f32x4 acc[2] = {};
    const int xn = nt * 16 + n16, xs = (xn >> 1) & 7;
    LAS char* xr0 = L + L_X + (xn * 72 + (g ^ xs)) * 16;
    LAS char* xr1 = L + L_X + (xn * 72 + ((4 + g) ^ xs)) * 16;
    bf16x8 XB[18], WA[2][3][2];
#pragma unroll
    for (int ks = 0; ks < 18; ++ks) XB[ks] = *(LAS bf16x8*)(((ks & 1) ? xr1 : xr0) + (ks >> 1) * 128);
    auto rdw = [&](int grp) {
#pragma unroll
      for (int k = 0; k < 3; ++k)
#pragma unroll
        for (int u = 0; u < 2; ++u)
          WA[grp & 1][k][u] = CAT8(TRREAD(L + wad[0][u] + (grp * 3 + k) * 4096), TRREAD(L + wad[1][u] + (grp * 3 + k) * 4096));
    };
    rdw(0);
#pragma unroll
    for (int grp = 0; grp < 6; ++grp) {
      __builtin_amdgcn_sched_barrier(0);
      if (grp + 1 < 6) rdw(grp + 1);
      __builtin_amdgcn_sched_barrier(0);
#pragma unroll
      for (int k = 0; k < 3; ++k)
#pragma unroll
        for (int u = 0; u < 2; ++u) acc[u] = MFMA(WA[grp & 1][k][u], XB[grp * 3 + k], acc[u]);
    }
    __builtin_amdgcn_sched_barrier(0);
    ++barv; lds_drain(); lds_st(a_bar + c * 4, barv);
    TS(11);
    const __amdgpu_buffer_rsrc_t rq = __builtin_amdgcn_make_buffer_rsrc(ws + WS_QM, 0, QM_BYTES, 0x00020000);
    {
      const int dbp = rg * 4 + nt * 2 + (n16 >> 3), dbid = (dbp << 4) | (bid & 15);
#pragma unroll
      for (int u = 0; u < 2; ++u) {
        const f32x4 v = acc[u];
        bf16x4 h; h[0] = (__bf16)v[0]; h[1] = (__bf16)v[1]; h[2] = (__bf16)v[2]; h[3] = (__bf16)v[3];
        const u32x2 w = __builtin_bit_cast(u32x2, h);
        const u32x4 chk = {w[0], w[1], w[0] ^ T1, w[1] ^ T2};
        __builtin_amdgcn_raw_buffer_store_b128(chk, rq, dbid * 8192 + (cb * 128 + (2 * ch + u) * 32 + (n16 & 7) * 4 + g) * 16, 0, 16);
      }
    }
    TS(12);
    {
      __builtin_amdgcn_s_sleep(PDELAY);
      u32x4 v0, v1; const unsigned off = bid * 8192 + (c * 128 + lane) * 16;
      const unsigned long long t0 = __builtin_amdgcn_s_memrealtime();
      for (;;) {
        asm volatile("" ::: "memory");
        v0 = __builtin_amdgcn_raw_buffer_load_b128(rq, off, 0, 16);
        v1 = __builtin_amdgcn_raw_buffer_load_b128(rq, off + 1024, 0, 16);
        const bool ok = v0[2] == (v0[0] ^ T1) && v0[3] == (v0[1] ^ T2) && v1[2] == (v1[0] ^ T1) && v1[3] == (v1[1] ^ T2);
        if (__all(ok)) break;
        if ((long long)(__builtin_amdgcn_s_memrealtime() - t0) > SPIN_CAP) break;
        __builtin_amdgcn_s_sleep(2);
      }
      TS(14);
      const int row = (lane >> 2) & 7, gg = lane & 3, r = c >> 1;
      LAS char* qd = L + L_QO + ((row >> 2) * 8 + r * 4 + (row & 3)) * QO_PITCH + (64 * (c & 1) + 16 * (lane >> 5) + 4 * gg) * 2;
      *(LAS u32x2*)(qd) = u32x2{v0[0], v0[1]};
      *(LAS u32x2*)(qd + 64) = u32x2{v1[0], v1[1]};
    }
    cbar();
    if (cb == 0 && c == 0 && lane == 0) __hip_atomic_store((unsigned*)(ws + WS_EQ + (kvh * 4 + rg) * 128), eq + 1, __ATOMIC_RELAXED, __HIP_MEMORY_SCOPE_AGENT);
  }
  TS(2);
  const unsigned tq = (unsigned)__builtin_amdgcn_s_memrealtime();
  unsigned kad[4];
#pragma unroll
  for (int s = 0; s < 4; ++s) kad[s] = n16 * 256 + (((4 * s + g) ^ n16) << 4);
  const unsigned mlo = n16 < 8 ? 0xffffffffu : 0u;
  int slot = c;
  int mi = c;
  auto next_tile = [&]() -> LAS char* {
    wait_full(mi);
    LAS char* tb = L + slot * TILE;
    const int mn = mi + 4;
    if (mn < N_SW) { slot += 4; if (slot >= NSLOT_A) slot -= NSLOT_A; }
    else if (mi < N_SW) slot = mn - N_SW;
    else { slot += 4; if (slot >= NSLOT_B) slot -= NSLOT_B; }
    mi = mn;
    return tb;
  };
  bf16x8 WF[8][3];
  const int wsb = min(768 + 96 * c, 1024);
  const unsigned wsof = (lane >> 3) * (DM * 2) + (lane & 7) * 16;
  const unsigned wrd = L_SCR + c * SCR_W + vrow * 128 + (768 + 96 * c - wsb) + (((lane & 3) >> 1) << 4) + ((lane & 1) << 3);
  auto wo_issue = [&](int ks) {
    const char* src = wo_h + (size_t)(32 * ks) * (DM * 2) + wsb;
#pragma unroll
    for (int i = 0; i < 4; ++i) dma1(wsof, src + (size_t)(8 * i) * (DM * 2), lb + L_SCR + c * SCR_W + i * 1024);
  };
  auto wo_read = [&](int ks) {
    wait_vm<0>();
#pragma unroll
    for (int mt = 0; mt < 3; ++mt) WF[ks][mt] = CAT8(TRREAD(L + wrd + mt * 32), TRREAD(L + wrd + mt * 32 + 2048));
    lds_drain();
  };
  auto pair = [&](auto PC) {
    constexpr int p = decltype(PC)::value;
    bf16x8 QA[4], QB[4];
#pragma unroll
    for (int s = 0; s < 4; ++s) {
      const u32x4 v = *(LAS u32x4*)(L + L_QO + (p * 8 + (n16 & 7)) * QO_PITCH + (4 * s + g) * 16);
      QA[s] = __builtin_bit_cast(bf16x8, v & mlo); QB[s] = __builtin_bit_cast(bf16x8, v & ~mlo);
    }
    f32x4 S[8][2];
#pragma unroll
    for (int i = 0; i < 8; ++i) {
      S[i][0] = f32x4{0.f, 0.f, 0.f, 0.f}; S[i][1] = S[i][0];
#pragma unroll
      for (int h = 0; h < 2; ++h) {
        LAS char* tb = next_tile();
        bf16x8 kf[2][4];
#pragma unroll
        for (int rb = 0; rb < 2; ++rb)
#pragma unroll
          for (int s = 0; s < 4; ++s) kf[rb][s] = *(LAS bf16x8*)(tb + kad[s] + rb * 4096);
        ++done; lds_st(a_done + c * 4, done);
#pragma unroll
        for (int s = 0; s < 4; ++s)
#pragma unroll
          for (int rb = 0; rb < 2; ++rb) S[i][rb] = MFMA(kf[rb][s], h ? QB[s] : QA[s], S[i][rb]);
      }
    }
    if (p == 0) TS(3); else TS(6);
    float m = S[0][0][0];
#pragma unroll
    for (int i = 0; i < 8; ++i)
#pragma unroll
      for (int rb = 0; rb < 2; ++rb)
#pragma unroll
        for (int j = 0; j < 4; ++j) m = fmaxf(m, S[i][rb][j]);
    m = fmaxf(m, dppf<0x128>(m));
    m = xrow_max(m);
    const float mc = m * C2;
    float l = 0.f;
    u32x4 P[8];
#pragma unroll
    for (int i = 0; i < 8; ++i) {
      bf16x8 pk;
#pragma unroll
      for (int rb = 0; rb < 2; ++rb)
#pragma unroll
        for (int j = 0; j < 4; ++j) { const float e = __builtin_amdgcn_exp2f(fmaf(S[i][rb][j], C2, -mc)); l += e; pk[rb * 4 + j] = (__bf16)e; }
      P[i] = __builtin_bit_cast(u32x4, pk);
    }
    l += dppf<0x128>(l);
    l = xrow_sum(l);
    if (lane < 8) { *(LAS float*)(L + L_ML + (c * 8 + lane) * 8) = mc; *(LAS float*)(L + L_ML + (c * 8 + lane) * 8 + 4) = l; }
    f32x4 O[8];
#pragma unroll
    for (int cb = 0; cb < 8; ++cb) O[cb] = f32x4{0.f, 0.f, 0.f, 0.f};
#pragma unroll
    for (int i = 0; i < 8; ++i) {
      if (p == 1) { if (i > 0) wo_read(i - 1); wo_issue(i); }
#pragma unroll
      for (int h = 0; h < 2; ++h) {
        const bf16x8 pb = __builtin_bit_cast(bf16x8, P[i] & (h ? ~mlo : mlo));
        if (p == 1 && i == 7 && h == 1) wo_read(7);
        LAS char* tb = next_tile();
        bf16x8 vf[8];
#pragma unroll
        for (int cb = 0; cb < 8; ++cb) vf[cb] = CAT8(TRREAD(tb + (vad0 ^ (cb << 5))), TRREAD(tb + (vad0 ^ (cb << 5)) + 4096));
        ++done; lds_st(a_done + c * 4, done);
#pragma unroll
        for (int cb = 0; cb < 8; ++cb) O[cb] = MFMA(vf[cb], pb, O[cb]);
      }
    }
    if (p == 0) TS(4); else TS(7);
#pragma unroll
    for (int cb = 0; cb < 8; ++cb)
#pragma unroll
      for (int j = 0; j < 4; ++j) O[cb][j] += dppf<0x128>(O[cb][j]);
    {
      LAS char* dst = L + L_SCR + c * SCR_W + (n16 & 7) * SCR_PITCH + g * 16 + (n16 >> 3) * 256;
#pragma unroll
      for (int cb = 0; cb < 4; ++cb) *(LAS f32x4*)(dst + cb * 64) = (n16 >> 3) ? O[4 + cb] : O[cb];
    }
    cbar();
    {
      const int tid = c * 64 + lane, q = tid >> 5, d4 = (tid & 31) * 4;
      float mw[4], lw[4];
#pragma unroll
      for (int w = 0; w < 4; ++w) { mw[w] = *(LAS float*)(L + L_ML + (w * 8 + q) * 8); lw[w] = *(LAS float*)(L + L_ML + (w * 8 + q) * 8 + 4); }
      const float M = fmaxf(fmaxf(mw[0], mw[1]), fmaxf(mw[2], mw[3]));
      float Ls = 0.f; f32x4 o = {0.f, 0.f, 0.f, 0.f};
#pragma unroll
      for (int w = 0; w < 4; ++w) { const float f = __builtin_amdgcn_exp2f(mw[w] - M); Ls += f * lw[w];
        const f32x4 v = *(LAS f32x4*)(L + L_SCR + w * SCR_W + q * SCR_PITCH + d4 * 4); o += v * f; }
      const float inv = 1.f / Ls;
      bf16x4 ob; ob[0] = (__bf16)(o[0] * inv); ob[1] = (__bf16)(o[1] * inv); ob[2] = (__bf16)(o[2] * inv); ob[3] = (__bf16)(o[3] * inv);
      *(LAS bf16x4*)(L + L_QO + (p * 8 + q) * QO_PITCH + d4 * 2) = ob;
    }
    cbar();
    if (p == 0) TS(5); else TS(8);
  };
  pair(std::integral_constant<int, 0>{});
  pair(std::integral_constant<int, 1>{});
  out_stage<3>(L, lb, WF, 384 + 48 * c, wave, lane, bid, kvh, bp, ws, out);
  TS(15);
  { unsigned t1 = (unsigned)__builtin_amdgcn_s_memrealtime(); unsigned long long c1 = __builtin_amdgcn_s_memtime(); if (wave == 4 && lane == 0 && (bid & 31) == 0) printf("bid %d start %u dur %u ticks  cycles %llu  tq %u\n", bid, tstart, t1 - tstart, c1 - cstart, tq - tstart); }
  TS_DUMP();
}
}

extern "C" void kernel_launch(void* const* d_in, const int* in_sizes, int n_in, void* d_out, int out_size, void* d_ws, size_t ws_size, hipStream_t stream) {
  using namespace gqa;
  (void)hipFuncSetAttribute((const void*)attn_kernel, hipFuncAttributeMaxDynamicSharedMemorySize, LDS_TOTAL);
  hipLaunchKernelGGL(attn_kernel, dim3(256), dim3(512), LDS_TOTAL, stream, (const char*)d_in[0], (const char*)d_in[1], (const char*)d_in[2], (const char*)d_in[3],
                     (const char*)d_in[4], (char*)d_out, (char*)d_ws);
}
```

```cpp
#include <hip/hip_runtime.h>
#include <stdint.h>
#include <type_traits>

namespace gqa {
typedef __bf16 bf16x8 __attribute__((ext_vector_type(8)));
typedef __bf16 bf16x4 __attribute__((ext_vector_type(4)));
typedef __bf16 bf16x2 __attribute__((ext_vector_type(2)));
typedef float f32x4 __attribute__((ext_vector_type(4)));
typedef float f32x2 __attribute__((ext_vector_type(2)));
typedef unsigned u32x4 __attribute__((ext_vector_type(4)));
typedef unsigned u32x2 __attribute__((ext_vector_type(2)));
#define LAS __attribute__((address_space(3)))

constexpr int SQ = 4, DM = 576, HKV = 16, SK = 2048, DH = 128, NQ = 4096;
#define INFL 4
#define KDELAY -1
#define ST_AUX 18
#define LD_AUX 18
#define PSLEEP 8
#define PDELAY 12
#define TAU16 448
#define TMIN16 428
#define TBURST 60
#define TORG 330
constexpr int TILE = 8192, NTILE = 256;
constexpr int NSLOT_A = 17, NSLOT_B = 9, N_SW = 128;
constexpr unsigned L_W = 4 * TILE, L_X = 13 * TILE;
constexpr unsigned L_PARK = NSLOT_B * TILE;
constexpr int N_FREE0 = 4;
constexpr unsigned L_SCR = NSLOT_A * TILE, SCR_PITCH = 528, SCR_W = 8 * SCR_PITCH;
constexpr unsigned QO_PITCH = 272;
constexpr unsigned L_QO = L_SCR + 17408;
constexpr unsigned L_ML = L_QO + 16 * QO_PITCH;
constexpr unsigned L_FLG = L_ML + 256;
constexpr unsigned LDS_BYTES = L_FLG + 128;
constexpr float C2 = 0.08838834764831845f * 1.4426950408889634f;
constexpr size_t WS_EQ = 0, WS_EO = 64 * 128, WS_QM = 65536, WS_OM = 4u << 20;
constexpr unsigned QM_BYTES = 256 * 8192, OM_STRIDE = 2304 * 16, OM_BYTES = 256 * OM_STRIDE;
constexpr long long SPIN_CAP = 2000000;

__device__ __forceinline__ unsigned lds_ld(unsigned addr) {
  unsigned v; asm volatile("ds_read_b32 %0, %1\n\ts_waitcnt lgkmcnt(0)" : "=v"(v) : "v"(addr) : "memory");
  return __builtin_amdgcn_readfirstlane(v);
}
__device__ __forceinline__ u32x4 lds_ld4(unsigned addr) {
  u32x4 v; asm volatile("ds_read_b128 %0, %1\n\ts_waitcnt lgkmcnt(0)" : "=v"(v) : "v"(addr) : "memory");
  u32x4 r; r[0] = __builtin_amdgcn_readfirstlane(v[0]); r[1] = __builtin_amdgcn_readfirstlane(v[1]);
  r[2] = __builtin_amdgcn_readfirstlane(v[2]); r[3] = __builtin_amdgcn_readfirstlane(v[3]); return r;
}
__device__ __forceinline__ void lds_st(unsigned addr, unsigned val) {
  asm volatile("ds_write_b32 %0, %1" :: "v"(addr), "v"(val) : "memory");
}
__device__ __forceinline__ void lds_drain() { asm volatile("s_waitcnt lgkmcnt(0)" ::: "memory"); }
__device__ __forceinline__ void dma8(unsigned o0, unsigned o1, unsigned o2, unsigned o3, const char* src, const char* src2, unsigned lds) {
  asm volatile("s_mov_b32 m0, %6\n\ts_nop 0\n\t"
               "global_load_lds_dwordx4 %0, %4 nt\n\t"
               "global_load_lds_dwordx4 %1, %4 offset:1024 nt\n\t"
               "global_load_lds_dwordx4 %2, %4 offset:2048 nt\n\t"
               "global_load_lds_dwordx4 %3, %4 offset:3072 nt\n\t"
               "s_add_u32 m0, m0, 0x1000\n\ts_nop 0\n\t"
               "global_load_lds_dwordx4 %0, %5 nt\n\t"
               "global_load_lds_dwordx4 %1, %5 offset:1024 nt\n\t"
               "global_load_lds_dwordx4 %2, %5 offset:2048 nt\n\t"
               "global_load_lds_dwordx4 %3, %5 offset:3072 nt"
               :: "v"(o0), "v"(o1), "v"(o2), "v"(o3), "s"(src), "s"(src2), "s"(lds) : "memory", "scc");
}
__device__ __forceinline__ void dma1(unsigned o, const char* src, unsigned lds) {
  asm volatile("s_mov_b32 m0, %2\n\ts_nop 0\n\tglobal_load_lds_dwordx4 %0, %1 nt" :: "v"(o), "s"(src), "s"(lds) : "memory");
}
#define WOPOL " nt"
#define WO_T0 4000
#define WO_DT 130
#define WOROT 0
__device__ __forceinline__ void dma1w(unsigned o, const char* src, unsigned lds) {
  asm volatile("s_mov_b32 m0, %2\n\ts_nop 0\n\tglobal_load_lds_dwordx4 %0, %1" WOPOL :: "v"(o), "s"(src), "s"(lds) : "memory");
}
template <int N> __device__ __forceinline__ void wait_vm() { asm volatile("s_waitcnt vmcnt(%0)" :: "n"(N) : "memory"); }
template <int CTRL> __device__ __forceinline__ float dppf(float x) {
  return __builtin_bit_cast(float, __builtin_amdgcn_mov_dpp(__builtin_bit_cast(int, x), CTRL, 0xf, 0xf, true));
}
__device__ __forceinline__ float xrow_max(float x) {
  auto s = __builtin_amdgcn_permlane16_swap(__float_as_uint(x), __float_as_uint(x), false, false);
  x = fmaxf(__uint_as_float(s[0]), __uint_as_float(s[1]));
  auto t = __builtin_amdgcn_permlane32_swap(__float_as_uint(x), __float_as_uint(x), false, false);
  return fmaxf(__uint_as_float(t[0]), __uint_as_float(t[1]));
}
__device__ __forceinline__ float xrow_sum(float x) {
  auto s = __builtin_amdgcn_permlane16_swap(__float_as_uint(x), __float_as_uint(x), false, false);
  x = __uint_as_float(s[0]) + __uint_as_float(s[1]);
  auto t = __builtin_amdgcn_permlane32_swap(__float_as_uint(x), __float_as_uint(x), false, false);
  return __uint_as_float(t[0]) + __uint_as_float(t[1]);
}
__device__ __forceinline__ unsigned read_epoch(const char* p) {
  unsigned e; asm volatile("s_load_dword %0, %1, 0x0\n\ts_waitcnt lgkmcnt(0)" : "=s"(e) : "s"(p) : "memory"); return e;
}
__device__ __forceinline__ bool all_ge(const u32x4 v, unsigned k) { return v[0] >= k && v[1] >= k && v[2] >= k && v[3] >= k; }
#define MFMA(a, b, c) __builtin_amdgcn_mfma_f32_16x16x32_bf16((a), (b), (c), 0, 0, 0)
#define TRREAD(p) __builtin_amdgcn_ds_read_tr16_b64_v4bf16((LAS bf16x4*)(p))
#define CAT8(lo, hi) __builtin_shufflevector((lo), (hi), 0, 1, 2, 3, 4, 5, 6, 7)

constexpr unsigned LDS_TOTAL = LDS_BYTES;
#define TS(slot) do {} while (0)
#define TS_INIT() do {} while (0)
#define TS_DUMP() do {} while (0)

template <int NM>
__device__ __forceinline__ void out_stage(LAS char* L, unsigned lb, const bf16x8 (&WF)[8][NM], int col0, int wave, int lane, int bid, int kvh, int bp,
                                          char* __restrict__ ws, char* __restrict__ out) {
  const int n16 = lane & 15, g = lane >> 4;
  const unsigned eo = lds_ld(lb + L_FLG + 8);
  const unsigned T1 = 2 * eo + 3, T2 = T1 * 0x9E3779B1u;
  f32x4 acc[NM];
#pragma unroll
  for (int mt = 0; mt < NM; ++mt) acc[mt] = f32x4{0.f, 0.f, 0.f, 0.f};
  LAS char* ob = L + L_QO + ((n16 >> 2) & 1) * 8 * QO_PITCH + (n16 & 3) * QO_PITCH + g * 8;
  bf16x8 B[8];
#pragma unroll
  for (int ks = 0; ks < 8; ++ks) {
    const int kk = (ks + (WOROT ? (bp & 7) : 0)) & 7;
    LAS char* a = ob + (kk >> 2) * 4 * QO_PITCH + (kk & 3) * 64;
    B[ks] = CAT8(*(LAS bf16x4*)a, *(LAS bf16x4*)(a + 32));
  }
#pragma unroll
  for (int ks = 0; ks < 8; ++ks)
#pragma unroll
    for (int mt = 0; mt < NM; ++mt) acc[mt] = MFMA(WF[ks][mt], B[ks], acc[mt]);
  asm volatile("" :: "v"(acc[0]), "v"(acc[NM - 1]));
  TS(24);
  const __amdgpu_buffer_rsrc_t ro = __builtin_amdgcn_make_buffer_rsrc(ws + WS_OM, 0, OM_BYTES, 0x00020000);
#pragma unroll
  for (int mt = 0; mt < NM; mt += 2) {
    const bool two = mt + 1 < NM;
    u32x4 a = __builtin_bit_cast(u32x4, acc[mt]);
    if (two) {
      const u32x4 b = __builtin_bit_cast(u32x4, acc[mt + 1]);
#pragma unroll
      for (int i = 0; i < 4; ++i) a[i] = __builtin_amdgcn_update_dpp(a[i], b[i], 0x118, 0xf, 0xc, false);
    }
    const int cc = col0 + 16 * (mt + (n16 >> 3)) + 4 * g, j = cc / 36, q = (cc - 36 * j) >> 2;
    const int dbid = (bp << 4) | (j >> 1) | ((j & 1) << 3);
    const unsigned off = dbid * OM_STRIDE + (((kvh * 9 + q) * 2) * 8 + (n16 & 7)) * 16;
    if (two || n16 < 8) {
      __builtin_amdgcn_raw_buffer_store_b128(u32x4{a[0], a[1], a[0] ^ T1, a[1] ^ T2}, ro, off, 0, ST_AUX);
      __builtin_amdgcn_raw_buffer_store_b128(u32x4{a[2], a[3], a[2] ^ T1, a[3] ^ T2}, ro, off + 128, 0, ST_AUX);
    }
  }
  lds_st(lb + L_FLG + 80 + wave * 4, 2);
  if (wave < 5) { for (;;) { const u32x4 a = lds_ld4(lb + L_FLG + 80), b = lds_ld4(lb + L_FLG + 96); if (all_ge(a, 2) && all_ge(b, 2)) break; } }
  TS(25);
  const int t = wave * 64 + lane;
  if (wave < 5) {
    int npoll = 0;
    const bool act = t < 288;
    const int idx = act ? (t >> 1) : 0;
    const unsigned off = bid * OM_STRIDE + ((t & 1) * 8 * 144 + idx) * 16;
    u32x4 v[8];
    unsigned miss = 0xff;
    const unsigned long long t0 = __builtin_amdgcn_s_memrealtime();
    for (;;) {
      asm volatile("" ::: "memory");
#pragma unroll
      for (int s = 0; s < 8; ++s) if (miss & (1u << s)) v[s] = __builtin_amdgcn_raw_buffer_load_b128(ro, off + s * 144 * 16, 0, LD_AUX);
#pragma unroll
      for (int s = 0; s < 8; ++s) if (miss & (1u << s)) { if (__all(!act || (v[s][2] == (v[s][0] ^ T1) && v[s][3] == (v[s][1] ^ T2)))) miss &= ~(1u << s); }
      ++npoll;
      if (!miss) break;
      if ((long long)(__builtin_amdgcn_s_memrealtime() - t0) > SPIN_CAP) break;
      const int nm = __builtin_popcount(miss);
      if (nm > 4) __builtin_amdgcn_s_sleep(PSLEEP); else if (nm > 1) __builtin_amdgcn_s_sleep(PSLEEP / 4); else __builtin_amdgcn_s_sleep(1);
    }
    TS(26);
    float s0 = 0.f, s1 = 0.f;
#pragma unroll
    for (int s = 0; s < 8; ++s) { s0 += __uint_as_float(v[s][0]); s1 += __uint_as_float(v[s][1]); }
    const float o0 = dppf<0xB1>(s0), o1 = dppf<0xB1>(s1);
    if (act && !(t & 1)) {
      const int q = idx >> 4, half = (idx >> 3) & 1, n = idx & 7;
      bf16x2 r; r[0] = (__bf16)(s0 + o0); r[1] = (__bf16)(s1 + o1);
      *(bf16x2*)(out + ((size_t)((2 * bp + (n >> 2)) * SQ + (n & 3)) * DM + 36 * kvh + 4 * q + 2 * half) * 2) = r;
    }
    if (kvh == 0 && t == 0) __hip_atomic_store((unsigned*)(ws + WS_EO + bp * 128), eo + 1, __ATOMIC_RELAXED, __HIP_MEMORY_SCOPE_AGENT);
  }
}

__global__ __launch_bounds__(512, 2) void attn_kernel(const char* __restrict__ x, const char* __restrict__ Kc, const char* __restrict__ Vc,
                                                       const char* __restrict__ wq, const char* __restrict__ wo, char* __restrict__ out, char* __restrict__ ws) {
  extern __shared__ __attribute__((aligned(16))) char smem[];
  LAS char* const L = (LAS char*)smem;
  const unsigned lb = (unsigned)(uintptr_t)L;
  const int lane = threadIdx.x & 63, wave = __builtin_amdgcn_readfirstlane(threadIdx.x >> 6);
  const int bid = blockIdx.x;
  const int kvh = (bid & 7) * 2 + ((bid >> 3) & 1), bp = bid >> 4;
  const unsigned a_full = lb + L_FLG, a_done = lb + L_FLG + 16, a_bar = lb + L_FLG + 32, a_lf = lb + L_FLG + 48, a_iss = lb + L_FLG + 80, a_park = lb + L_FLG + 112;
  const int n16 = lane & 15, g = lane >> 4;
  const unsigned tstart = (unsigned)__builtin_amdgcn_s_memrealtime();
  TS_INIT();
  TS(0);
  if (wave == 0) { if (lane < 32) lds_st(lb + L_FLG + lane * 4, 0); lds_drain(); }
  __builtin_amdgcn_s_barrier();
  const unsigned vrow = 4 * g + (n16 >> 2);
  const unsigned vad0 = vrow * 256 + ((vrow & 7) << 5) + (((lane & 3) >> 1) << 4) + ((lane & 1) << 3);
  const char* wo_h = wo + (size_t)kvh * 256 * (DM * 2);
  const int krot = WOROT ? (bp & 7) : 0;
  if (wave == 0) {
    __builtin_amdgcn_s_setprio(3);
    const unsigned r4 = lane >> 4, p = lane & 15;
    unsigned ko[4], vo[2];
#pragma unroll
    for (int i = 0; i < 4; ++i) ko[i] = r4 * 256 + ((p ^ ((4 * i + r4) & 15)) << 4);
#pragma unroll
    for (int i = 0; i < 2; ++i) vo[i] = r4 * 256 + ((p ^ (((4 * i + r4) & 7) << 1)) << 4);
    const size_t pair0 = (size_t)((2 * bp) * HKV + kvh) * (SK * DH * 2), pstep = (size_t)HKV * (SK * DH * 2);
    for (;;) { const u32x4 a = lds_ld4(a_iss), b = lds_ld4(a_iss + 16); if (a[0] && a[1] && a[2] && a[3] && b[0] && b[1] && b[2]) break; }
    TS(2);
    unsigned due16 = (tstart + TORG) << 4, min16 = tstart << 4;
    int freeto = N_FREE0;
    int n = 0, pub = 0; unsigned dst = lb, ring_end = lb + NSLOT_A * TILE; int lap = NSLOT_A;
    auto refresh = [&]() {
      bool first = true;
      for (;;) {
        const u32x4 d = lds_ld4(a_done), b = lds_ld4(a_bar);
        int fr = 4 * d[0]; fr = min(fr, (int)(4 * d[1] + 1)); fr = min(fr, (int)(4 * d[2] + 2)); fr = min(fr, (int)(4 * d[3] + 3));
        freeto = all_ge(b, 1) ? fr + lap : N_FREE0;
        if (n < freeto) break;
        if (first) { wait_vm<0>(); pub = n; lds_st(a_full, n); first = false; TS(4); }
        __builtin_amdgcn_s_sleep(1);
      }
    };
    auto publish_landed = [&]() { if (pub < n - 1) { wait_vm<8>(); pub = n - 1; lds_st(a_full, pub); } else __builtin_amdgcn_s_sleep(1); };
#pragma unroll 1
    for (int seg = 0; seg < 4; ++seg) {
      const char* src = ((seg & 1) ? Vc : Kc) + pair0 + (seg >> 1) * pstep;
      const unsigned o0 = (seg & 1) ? vo[0] : ko[0], o1 = (seg & 1) ? vo[1] : ko[1], o2 = (seg & 1) ? vo[0] : ko[2], o3 = (seg & 1) ? vo[1] : ko[3];
      if (seg == 2) { dst = lb; ring_end = lb + NSLOT_B * TILE; freeto -= NSLOT_A - NSLOT_B; lap = NSLOT_B; }
#pragma unroll 1
      for (int t = 0; t < 64; ++t) {
        if (n >= freeto) refresh();
        {
          unsigned now = (unsigned)__builtin_amdgcn_s_memrealtime();
          const unsigned dd = (int)(due16 - min16) > 0 ? due16 : min16;
          while ((int)((now << 4) - dd) < 0) { publish_landed(); now = (unsigned)__builtin_amdgcn_s_memrealtime(); }
          const unsigned lo = (now - TBURST) << 4;
          if ((int)(lo - min16) > 0) min16 = lo;
          min16 += TMIN16; due16 += TAU16;
        }
        wait_vm<8 * (INFL - 1)>();
        dma8(o0, o1, o2, o3, src, src + 4096, dst);
        if (n + 1 - INFL > pub) { pub = n + 1 - INFL; lds_st(a_full, pub); }
        src += TILE; dst += TILE; if (dst == ring_end) dst = lb;
        ++n;
      }
    }
    while (!(lds_ld(a_park) && lds_ld(a_park + 4) && lds_ld(a_park + 8))) {}
    bf16x8 WF[8][6];
    auto rdpark = [&](int k0, int k1) {
#pragma unroll
      for (int ks = k0; ks < k1; ++ks)
#pragma unroll
        for (int mt = 0; mt < 6; ++mt) {
          LAS char* tb = L + L_PARK + ks * TILE + (vad0 ^ (mt << 5));
          WF[ks][mt] = CAT8(TRREAD(tb), TRREAD(tb + 4096));
        }
    };
    static_assert(INFL == 3 || INFL == 4, "drain ladder");
    if (INFL == 4) { wait_vm<24>(); if (pub < NTILE - 3) lds_st(a_full, NTILE - 3); }
    rdpark(0, 3); lds_drain(); wait_vm<16>(); if (pub < NTILE - 2) lds_st(a_full, NTILE - 2);
    rdpark(3, 6); lds_drain(); wait_vm<8>(); lds_st(a_full, NTILE - 1);
    wait_vm<0>(); lds_st(a_full, NTILE);
    TS(1);
    rdpark(6, 8);
    lds_drain(); __builtin_amdgcn_s_barrier(); __builtin_amdgcn_s_barrier();
    TS(3);
    out_stage<6>(L, lb, WF, 288, wave, lane, bid, kvh, bp, ws, out);
    TS(6);
    TS_DUMP();
    return;
  }
  if (wave < 4) {
    const int h = wave - 1;
    {
      unsigned wo2[2];
#pragma unroll
      for (int i = 0; i < 2; ++i) { const unsigned ri = lane >> 3, f = ((ri >> 1) & 1) | (i << 1); wo2[i] = ri * (NQ * 2) + (((lane & 7) ^ (f << 1)) << 4); }
      const char* wb = wq + (size_t)kvh * 512 + (bp & 3) * 128 + (size_t)(192 * h) * (NQ * 2);
      const unsigned dst0 = lb + L_W + h * 24 * 1024;
      int ii = 3 * (bp >> 2);
#pragma unroll 1
      for (int i = 0; i < 12; ++i) {
        const char* w1 = wb + (size_t)ii * (16 * NQ * 2); const unsigned dst = dst0 + ii * 2048;
        dma1(wo2[0], w1, dst); dma1(wo2[1], w1 + 8 * NQ * 2, dst + 1024);
        ++ii; if (ii == 12) ii = 0;
      }
      lds_st(a_iss + h * 4, 1);
      if (h == 0) {
        const unsigned eq = read_epoch(ws + WS_EQ + (kvh * 4 + (bp >> 2)) * 128), eo = read_epoch(ws + WS_EO + bp * 128);
        lds_st(a_full + 4, eq); lds_st(a_full + 8, eo);
      }
      TS(1);
      wait_vm<0>(); lds_st(a_lf + h * 4, 1);
      TS(3);
    }
    for (;;) { const u32x4 d = lds_ld4(a_done); if (all_ge(d, 30)) break; __builtin_amdgcn_s_sleep(32); }
    TS(4);
    unsigned wv[2];
    { const unsigned r4 = lane >> 4, p = lane & 15;
#pragma unroll
      for (int i = 0; i < 2; ++i) wv[i] = r4 * (DM * 2) + ((p ^ (((4 * i + r4) & 7) << 1)) << 4); }
    unsigned wdue = tstart + WO_T0 + h * (WO_DT / 3);
    auto ld_tile = [&](const char* src, unsigned dst) {
#pragma unroll 1
      for (int i = 0; i < 8; i += 2) {
        if (WO_DT > 0 && !(i & 2)) {
          while ((int)((unsigned)__builtin_amdgcn_s_memrealtime() - wdue) < 0) __builtin_amdgcn_s_sleep(1);
          wdue += WO_DT;
        }
        dma1w(wv[0], src + (size_t)(4 * i) * (DM * 2), dst + i * 1024); dma1w(wv[1], src + (size_t)(4 * i + 4) * (DM * 2), dst + (i + 1) * 1024);
        if (i & 2) wait_vm<0>();
      }
    };
    bf16x8 WF[8][6];
    const unsigned stg = L_PARK + h * TILE;
#pragma unroll
    for (int ks = 0; ks < 8; ++ks) {
      ld_tile(wo_h + (size_t)(32 * ((ks + krot) & 7)) * (DM * 2) + 192 * h, lb + stg);
#pragma unroll
      for (int mt = 0; mt < 6; ++mt) { LAS char* tb = L + stg + (vad0 ^ (mt << 5)); WF[ks][mt] = CAT8(TRREAD(tb), TRREAD(tb + 4096)); }
      lds_drain();
    }
#pragma unroll 1
    for (int ks = h; ks < 8; ks += 3) ld_tile(wo_h + (size_t)(32 * ((ks + krot) & 7)) * (DM * 2) + 576, lb + L_PARK + ks * TILE);
    lds_st(a_park + h * 4, 1);
    TS(5);
    __builtin_amdgcn_s_barrier(); __builtin_amdgcn_s_barrier();
    TS(6);
    out_stage<6>(L, lb, WF, 96 * h, wave, lane, bid, kvh, bp, ws, out);
    TS(7);
    TS_DUMP(); return;
  }
  const int c = wave - 4;
  unsigned barv = 0;
  auto cbar = [&]() {
    ++barv; lds_drain();
    lds_st(a_bar + c * 4, barv);
    while (!all_ge(lds_ld4(a_bar), barv)) {}
  };
  unsigned full = 0, done = 0;
  auto wait_full = [&](unsigned m) { while (full <= m) { full = lds_ld(a_full); if (full <= m) __builtin_amdgcn_s_sleep(1); } };
  {
    const int rg = bp >> 2, cb = bp & 3, nt = c & 1, ch = c >> 1;
    {
      const char* xb = x + (size_t)(rg * 32) * (DM * 2);
      unsigned dst = lb + L_X + c * 9 * 1024;
      unsigned P = c * 9 * 64 + lane;
#pragma unroll 1
      for (int i = 0; i < 9; ++i) {
        const unsigned n = (P * 58255u) >> 22;
        dma1((P ^ ((n >> 1) & 7)) << 4, xb, dst);
        P += 64; dst += 1024;
      }
      lds_st(a_iss + 12 + c * 4, 1);
    }
    TS(9);
    wait_vm<0>(); lds_st(a_lf + 12 + c * 4, 1);
    TS(10);
    for (;;) { const u32x4 a = lds_ld4(a_lf), b = lds_ld4(a_lf + 16); if (a[0] && a[1] && a[2] && a[3] && b[0] && b[1] && b[2]) break; }
    TS(13);
    const unsigned eq = lds_ld(a_full + 4);
    const unsigned T1 = 2 * eq + 3, T2 = T1 * 0x9E3779B1u;
    const int q4 = n16 >> 2, pp = lane & 3;
    unsigned wad[2][2];
#pragma unroll
    for (int t = 0; t < 2; ++t) { const int row = 8 * g + 4 * t + q4, f = ((row >> 1) & 1) | ((g & 1) << 1);
#pragma unroll
      for (int u = 0; u < 2; ++u) wad[t][u] = L_W + row * 128 + ((f ^ (2 * ch + u)) << 5) + ((pp >> 1) << 4) + ((pp & 1) << 3); }
    f32x4 acc[2] = {};
    const int xn = nt * 16 + n16, xs = (xn >> 1) & 7;
    LAS char* xr0 = L + L_X + (xn * 72 + (g ^ xs)) * 16;
    LAS char* xr1 = L + L_X + (xn * 72 + ((4 + g) ^ xs)) * 16;
    bf16x8 XB[18], WA[2][3][2];
#pragma unroll
    for (int ks = 0; ks < 18; ++ks) XB[ks] = *(LAS bf16x8*)(((ks & 1) ? xr1 : xr0) + (ks >> 1) * 128);
    auto rdw = [&](int grp) {
#pragma unroll
      for (int k = 0; k < 3; ++k)
#pragma unroll
        for (int u = 0; u < 2; ++u)
          WA[grp & 1][k][u] = CAT8(TRREAD(L + wad[0][u] + (grp * 3 + k) * 4096), TRREAD(L + wad[1][u] + (grp * 3 + k) * 4096));
    };
    rdw(0);
#pragma unroll
    for (int grp = 0; grp < 6; ++grp) {
      __builtin_amdgcn_sched_barrier(0);
      if (grp + 1 < 6) rdw(grp + 1);
      __builtin_amdgcn_sched_barrier(0);
#pragma unroll
      for (int k = 0; k < 3; ++k)
#pragma unroll
        for (int u = 0; u < 2; ++u) acc[u] = MFMA(WA[grp & 1][k][u], XB[grp * 3 + k], acc[u]);
    }
    __builtin_amdgcn_sched_barrier(0);
    ++barv; lds_drain(); lds_st(a_bar + c * 4, barv);
    TS(11);
    const __amdgpu_buffer_rsrc_t rq = __builtin_amdgcn_make_buffer_rsrc(ws + WS_QM, 0, QM_BYTES, 0x00020000);
    {
      const int dbp = rg * 4 + nt * 2 + (n16 >> 3), dbid = (dbp << 4) | (bid & 15);
#pragma unroll
      for (int u = 0; u < 2; ++u) {
        const f32x4 v = acc[u];
        bf16x4 h; h[0] = (__bf16)v[0]; h[1] = (__bf16)v[1]; h[2] = (__bf16)v[2]; h[3] = (__bf16)v[3];
        const u32x2 w = __builtin_bit_cast(u32x2, h);
        const u32x4 chk = {w[0], w[1], w[0] ^ T1, w[1] ^ T2};
        __builtin_amdgcn_raw_buffer_store_b128(chk, rq, dbid * 8192 + (cb * 128 + (2 * ch + u) * 32 + (n16 & 7) * 4 + g) * 16, 0, ST_AUX);
      }
    }
    TS(12);
    {
      __builtin_amdgcn_s_sleep(PDELAY);
      u32x4 v0, v1; const unsigned off = bid * 8192 + (c * 128 + lane) * 16;
      const unsigned long long t0 = __builtin_amdgcn_s_memrealtime();
      for (;;) {
        asm volatile("" ::: "memory");
        v0 = __builtin_amdgcn_raw_buffer_load_b128(rq, off, 0, LD_AUX);
        v1 = __builtin_amdgcn_raw_buffer_load_b128(rq, off + 1024, 0, LD_AUX);
        const bool ok = v0[2] == (v0[0] ^ T1) && v0[3] == (v0[1] ^ T2) && v1[2] == (v1[0] ^ T1) && v1[3] == (v1[1] ^ T2);
        if (__all(ok)) break;
        if ((long long)(__builtin_amdgcn_s_memrealtime() - t0) > SPIN_CAP) break;
        __builtin_amdgcn_s_sleep(2);
      }
      TS(14);
      const int row = (lane >> 2) & 7, gg = lane & 3, r = c >> 1;
      LAS char* qd = L + L_QO + ((row >> 2) * 8 + r * 4 + (row & 3)) * QO_PITCH + (64 * (c & 1) + 16 * (lane >> 5) + 4 * gg) * 2;
      *(LAS u32x2*)(qd) = u32x2{v0[0], v0[1]};
      *(LAS u32x2*)(qd + 64) = u32x2{v1[0], v1[1]};
    }
    cbar();
    if (cb == 0 && c == 0 && lane == 0) __hip_atomic_store((unsigned*)(ws + WS_EQ + (kvh * 4 + rg) * 128), eq + 1, __ATOMIC_RELAXED, __HIP_MEMORY_SCOPE_AGENT);
  }
  TS(2);
  unsigned kad[4];
#pragma unroll
  for (int s = 0; s < 4; ++s) kad[s] = n16 * 256 + (((4 * s + g) ^ n16) << 4);
  const unsigned mlo = n16 < 8 ? 0xffffffffu : 0u;
  int slot = c;
  int mi = c;
  auto next_tile = [&]() -> LAS char* {
    wait_full(mi);
    LAS char* tb = L + slot * TILE;
    const int mn = mi + 4;
    if (mn < N_SW) { slot += 4; if (slot >= NSLOT_A) slot -= NSLOT_A; }
    else if (mi < N_SW) slot = mn - N_SW;
    else { slot += 4; if (slot >= NSLOT_B) slot -= NSLOT_B; }
    mi = mn;
    return tb;
  };
  bf16x8 WF[8][3];
  const int wsb = min(768 + 96 * c, 1024);
  const unsigned wsof = (lane >> 3) * (DM * 2) + (lane & 7) * 16;
  const unsigned wrd = L_SCR + c * SCR_W + vrow * 128 + (768 + 96 * c - wsb) + (((lane & 3) >> 1) << 4) + ((lane & 1) << 3);
  auto wo_issue = [&](int ks) {
    const char* src = wo_h + (size_t)(32 * ((ks + krot) & 7)) * (DM * 2) + wsb;
#pragma unroll
    for (int i = 0; i < 4; ++i) dma1w(wsof, src + (size_t)(8 * i) * (DM * 2), lb + L_SCR + c * SCR_W + i * 1024);
  };
  auto wo_read = [&](int ks) {
    wait_vm<0>();
#pragma unroll
    for (int mt = 0; mt < 3; ++mt) WF[ks][mt] = CAT8(TRREAD(L + wrd + mt * 32), TRREAD(L + wrd + mt * 32 + 2048));
    lds_drain();
  };
  auto pair = [&](auto PC) {
    constexpr int p = decltype(PC)::value;
    bf16x8 QA[4], QB[4];
#pragma unroll
    for (int s = 0; s < 4; ++s) {
      const u32x4 v = *(LAS u32x4*)(L + L_QO + (p * 8 + (n16 & 7)) * QO_PITCH + (4 * s + g) * 16);
      QA[s] = __builtin_bit_cast(bf16x8, v & mlo); QB[s] = __builtin_bit_cast(bf16x8, v & ~mlo);
    }
    f32x4 S[8][2];
#pragma unroll
    for (int i = 0; i < 8; ++i) {
      S[i][0] = f32x4{0.f, 0.f, 0.f, 0.f}; S[i][1] = S[i][0];
#pragma unroll
      for (int h = 0; h < 2; ++h) {
        LAS char* tb = next_tile();
        bf16x8 kf[2][4];
#pragma unroll
        for (int rb = 0; rb < 2; ++rb)
#pragma unroll
          for (int s = 0; s < 4; ++s) kf[rb][s] = *(LAS bf16x8*)(tb + kad[s] + rb * 4096);
        ++done; lds_st(a_done + c * 4, done);
#pragma unroll
        for (int s = 0; s < 4; ++s)
#pragma unroll
          for (int rb = 0; rb < 2; ++rb) S[i][rb] = MFMA(kf[rb][s], h ? QB[s] : QA[s], S[i][rb]);
      }
    }
    if (p == 0) TS(3); else TS(6);
    float m = S[0][0][0];
#pragma unroll
    for (int i = 0; i < 8; ++i)
#pragma unroll
      for (int rb = 0; rb < 2; ++rb)
#pragma unroll
        for (int j = 0; j < 4; ++j) m = fmaxf(m, S[i][rb][j]);
    m = fmaxf(m, dppf<0x128>(m));
    m = xrow_max(m);
    const float mc = m * C2;
    float l = 0.f;
#pragma unroll
    for (int i = 0; i < 8; ++i)
#pragma unroll
      for (int rb = 0; rb < 2; ++rb)
#pragma unroll
        for (int j = 0; j < 4; ++j) { const float e = __builtin_amdgcn_exp2f(fmaf(S[i][rb][j], C2, -mc)); l += e; S[i][rb][j] = e; }
    l += dppf<0x128>(l);
    l = xrow_sum(l);
    if (lane < 8) *(LAS f32x2*)(L + L_ML + (c * 8 + lane) * 8) = f32x2{mc, l};
    cbar();
    float scale;
    {
      f32x2 ml[4];
#pragma unroll
      for (int w = 0; w < 4; ++w) ml[w] = *(LAS f32x2*)(L + L_ML + (w * 8 + (n16 & 7)) * 8);
      const float M = fmaxf(fmaxf(ml[0][0], ml[1][0]), fmaxf(ml[2][0], ml[3][0]));
      float Ls = 0.f;
#pragma unroll
      for (int w = 0; w < 4; ++w) Ls = fmaf(__builtin_amdgcn_exp2f(ml[w][0] - M), ml[w][1], Ls);
      scale = __builtin_amdgcn_exp2f(mc - M) / Ls;
    }
    u32x4 P[8];
#pragma unroll
    for (int i = 0; i < 8; ++i) {
      bf16x8 pk;
#pragma unroll
      for (int rb = 0; rb < 2; ++rb)
#pragma unroll
        for (int j = 0; j < 4; ++j) pk[rb * 4 + j] = (__bf16)(S[i][rb][j] * scale);
      P[i] = __builtin_bit_cast(u32x4, pk);
    }
    f32x4 O[8];
#pragma unroll
    for (int cb = 0; cb < 8; ++cb) O[cb] = f32x4{0.f, 0.f, 0.f, 0.f};
#pragma unroll
    for (int i = 0; i < 8; ++i) {
      if (p == 1) { if (i > 0) wo_read(i - 1); wo_issue(i); }
#pragma unroll
      for (int h = 0; h < 2; ++h) {
        const bf16x8 pb = __builtin_bit_cast(bf16x8, P[i] & (h ? ~mlo : mlo));
        if (p == 1 && i == 7 && h == 1) wo_read(7);
        LAS char* tb = next_tile();
        bf16x8 vf[8];
#pragma unroll
        for (int cb = 0; cb < 8; ++cb) vf[cb] = CAT8(TRREAD(tb + (vad0 ^ (cb << 5))), TRREAD(tb + (vad0 ^ (cb << 5)) + 4096));
        ++done; lds_st(a_done + c * 4, done);
#pragma unroll
        for (int cb = 0; cb < 8; ++cb) O[cb] = MFMA(vf[cb], pb, O[cb]);
      }
    }
    if (p == 0) TS(4); else TS(7);
#pragma unroll
    for (int cb = 0; cb < 8; ++cb)
#pragma unroll
      for (int j = 0; j < 4; ++j) O[cb][j] += dppf<0x128>(O[cb][j]);
    {
      LAS char* dst = L + L_SCR + c * SCR_W + (n16 & 7) * SCR_PITCH + g * 16 + (n16 >> 3) * 256;
#pragma unroll
      for (int cb = 0; cb < 4; ++cb) *(LAS f32x4*)(dst + cb * 64) = (n16 >> 3) ? O[4 + cb] : O[cb];
    }
    if (p == 0) cbar(); else { lds_drain(); __builtin_amdgcn_s_barrier(); }
    {
      const int tid = c * 64 + lane, q = tid >> 5, d4 = (tid & 31) * 4;
      f32x4 v[4];
#pragma unroll
      for (int w = 0; w < 4; ++w) v[w] = *(LAS f32x4*)(L + L_SCR + w * SCR_W + q * SCR_PITCH + d4 * 4);
      const f32x4 o = (v[0] + v[1]) + (v[2] + v[3]);
      bf16x4 ob; ob[0] = (__bf16)o[0]; ob[1] = (__bf16)o[1]; ob[2] = (__bf16)o[2]; ob[3] = (__bf16)o[3];
      *(LAS bf16x4*)(L + L_QO + (p * 8 + q) * QO_PITCH + d4 * 2) = ob;
    }
    if (p == 0) cbar(); else { lds_drain(); __builtin_amdgcn_s_barrier(); }
    if (p == 0) TS(5); else TS(8);
  };
  pair(std::integral_constant<int, 0>{});
  pair(std::integral_constant<int, 1>{});
  out_stage<3>(L, lb, WF, 384 + 48 * c, wave, lane, bid, kvh, bp, ws, out);
  TS(15);
  TS_DUMP();
}
}

extern "C" void kernel_launch(void* const* d_in, const int* in_sizes, int n_in, void* d_out, int out_size, void* d_ws, size_t ws_size, hipStream_t stream) {
  using namespace gqa;
  (void)hipFuncSetAttribute((const void*)attn_kernel, hipFuncAttributeMaxDynamicSharedMemorySize, LDS_TOTAL);
  hipLaunchKernelGGL(attn_kernel, dim3(256), dim3(512), LDS_TOTAL, stream, (const char*)d_in[0], (const char*)d_in[1], (const char*)d_in[2], (const char*)d_in[3],
                     (const char*)d_in[4], (char*)d_out, (char*)d_ws);
}
```

```cpp
#include <hip/hip_runtime.h>
#include <stdint.h>
#include <type_traits>

namespace gqa {
typedef __bf16 bf16x8 __attribute__((ext_vector_type(8)));
typedef __bf16 bf16x4 __attribute__((ext_vector_type(4)));
typedef __bf16 bf16x2 __attribute__((ext_vector_type(2)));
typedef float f32x4 __attribute__((ext_vector_type(4)));
typedef float f32x2 __attribute__((ext_vector_type(2)));
typedef unsigned u32x4 __attribute__((ext_vector_type(4)));
typedef unsigned u32x2 __attribute__((ext_vector_type(2)));
#define LAS __attribute__((address_space(3)))

constexpr int SQ = 4, DM = 576, HKV = 16, SK = 2048, DH = 128, NQ = 4096;
#define INFL 4
#define KDELAY -1
#define ST_AUX 18
#define LD_AUX 18
#define QST_AUX 18
#define QLD_AUX 18
#define PD2 16
#define PSLEEP 8
#define PDELAY 28
#define TAU16 448
#define TMIN16 428
#define TBURST 60
#define TORG 300
constexpr int TILE = 8192, NTILE = 256;
constexpr int NSLOT_A = 17, NSLOT_B = 9, N_SW = 128;
constexpr unsigned L_W = 4 * TILE, L_X = 13 * TILE;
constexpr unsigned L_PARK = NSLOT_B * TILE;
constexpr int N_FREE0 = 4;
constexpr unsigned L_SCR = NSLOT_A * TILE, SCR_PITCH = 528, SCR_W = 8 * SCR_PITCH;
constexpr unsigned QO_PITCH = 272;
constexpr unsigned L_QO = L_SCR + 17408;
constexpr unsigned L_ML = L_QO + 16 * QO_PITCH;
constexpr unsigned L_FLG = L_ML + 512;
constexpr unsigned LDS_BYTES = L_FLG + 160;
constexpr float C2 = 0.08838834764831845f * 1.4426950408889634f;
constexpr size_t WS_EQ = 0, WS_EO = 64 * 128, WS_QM = 65536, WS_OM = 4u << 20;
constexpr unsigned QM_BYTES = 256 * 8192, OM_STRIDE = 1152 * 16, OM_BYTES = 256 * OM_STRIDE;
constexpr long long SPIN_CAP = 2000000;

__device__ __forceinline__ unsigned lds_ld(unsigned addr) {
  unsigned v; asm volatile("ds_read_b32 %0, %1\n\ts_waitcnt lgkmcnt(0)" : "=v"(v) : "v"(addr) : "memory");
  return __builtin_amdgcn_readfirstlane(v);
}
__device__ __forceinline__ u32x4 lds_ld4(unsigned addr) {
  u32x4 v; asm volatile("ds_read_b128 %0, %1\n\ts_waitcnt lgkmcnt(0)" : "=v"(v) : "v"(addr) : "memory");
  u32x4 r; r[0] = __builtin_amdgcn_readfirstlane(v[0]); r[1] = __builtin_amdgcn_readfirstlane(v[1]);
  r[2] = __builtin_amdgcn_readfirstlane(v[2]); r[3] = __builtin_amdgcn_readfirstlane(v[3]); return r;
}
__device__ __forceinline__ void lds_st(unsigned addr, unsigned val) {
  asm volatile("ds_write_b32 %0, %1" :: "v"(addr), "v"(val) : "memory");
}
__device__ __forceinline__ void lds_drain() { asm volatile("s_waitcnt lgkmcnt(0)" ::: "memory"); }
__device__ __forceinline__ void dma8(unsigned o0, unsigned o1, unsigned o2, unsigned o3, const char* src, const char* src2, unsigned lds) {
  asm volatile("s_mov_b32 m0, %6\n\ts_nop 0\n\t"
               "global_load_lds_dwordx4 %0, %4 nt\n\t"
               "global_load_lds_dwordx4 %1, %4 offset:1024 nt\n\t"
               "global_load_lds_dwordx4 %2, %4 offset:2048 nt\n\t"
               "global_load_lds_dwordx4 %3, %4 offset:3072 nt\n\t"
               "s_add_u32 m0, m0, 0x1000\n\ts_nop 0\n\t"
               "global_load_lds_dwordx4 %0, %5 nt\n\t"
               "global_load_lds_dwordx4 %1, %5 offset:1024 nt\n\t"
               "global_load_lds_dwordx4 %2, %5 offset:2048 nt\n\t"
               "global_load_lds_dwordx4 %3, %5 offset:3072 nt"
               :: "v"(o0), "v"(o1), "v"(o2), "v"(o3), "s"(src), "s"(src2), "s"(lds) : "memory", "scc");
}
__device__ __forceinline__ void dma1(unsigned o, const char* src, unsigned lds) {
  asm volatile("s_mov_b32 m0, %2\n\ts_nop 0\n\tglobal_load_lds_dwordx4 %0, %1 nt" :: "v"(o), "s"(src), "s"(lds) : "memory");
}
#define WOPOL " nt"
#define WO_T0 4150
#define WO_DT 130
__device__ __forceinline__ void dma1w(unsigned o, const char* src, unsigned lds) {
  asm volatile("s_mov_b32 m0, %2\n\ts_nop 0\n\tglobal_load_lds_dwordx4 %0, %1" WOPOL :: "v"(o), "s"(src), "s"(lds) : "memory");
}
template <int N> __device__ __forceinline__ void wait_vm() { asm volatile("s_waitcnt vmcnt(%0)" :: "n"(N) : "memory"); }
template <int CTRL> __device__ __forceinline__ float dppf(float x) {
  return __builtin_bit_cast(float, __builtin_amdgcn_mov_dpp(__builtin_bit_cast(int, x), CTRL, 0xf, 0xf, true));
}
__device__ __forceinline__ float xrow_max(float x) {
  auto s = __builtin_amdgcn_permlane16_swap(__float_as_uint(x), __float_as_uint(x), false, false);
  x = fmaxf(__uint_as_float(s[0]), __uint_as_float(s[1]));
  auto t = __builtin_amdgcn_permlane32_swap(__float_as_uint(x), __float_as_uint(x), false, false);
  return fmaxf(__uint_as_float(t[0]), __uint_as_float(t[1]));
}
__device__ __forceinline__ float xrow_sum(float x) {
  auto s = __builtin_amdgcn_permlane16_swap(__float_as_uint(x), __float_as_uint(x), false, false);
  x = __uint_as_float(s[0]) + __uint_as_float(s[1]);
  auto t = __builtin_amdgcn_permlane32_swap(__float_as_uint(x), __float_as_uint(x), false, false);
  return __uint_as_float(t[0]) + __uint_as_float(t[1]);
}
__device__ __forceinline__ unsigned read_epoch(const char* p) {
  unsigned e; asm volatile("s_load_dword %0, %1, 0x0\n\ts_waitcnt lgkmcnt(0)" : "=s"(e) : "s"(p) : "memory"); return e;
}
__device__ __forceinline__ unsigned rotl32(unsigned x, int r) { return __builtin_amdgcn_alignbit(x, x, 32 - r); }
__device__ __forceinline__ unsigned chk_pair(const unsigned w0, const unsigned w1, const unsigned w2, bool odd, unsigned T1, unsigned T2) {
  const unsigned p0 = __builtin_amdgcn_mov_dpp(w0, 0xB1, 0xf, 0xf, true), p1 = __builtin_amdgcn_mov_dpp(w1, 0xB1, 0xf, 0xf, true), p2 = __builtin_amdgcn_mov_dpp(w2, 0xB1, 0xf, 0xf, true);
  const unsigned a0 = odd ? p0 : w0, a1 = odd ? p1 : w1, a2 = odd ? p2 : w2, b0 = odd ? w0 : p0, b1 = odd ? w1 : p1, b2 = odd ? w2 : p2;
  const unsigned h1 = a0 ^ rotl32(a1, 5) ^ rotl32(a2, 11) ^ rotl32(b0, 17) ^ rotl32(b1, 23) ^ rotl32(b2, 29);
  const unsigned h2 = (rotl32(a0, 3) + rotl32(a1, 9) + rotl32(a2, 15)) ^ (rotl32(b0, 21) + rotl32(b1, 27) + b2);
  return odd ? (h2 ^ T2) : (h1 ^ T1);
}
__device__ __forceinline__ bool all_ge(const u32x4 v, unsigned k) { return v[0] >= k && v[1] >= k && v[2] >= k && v[3] >= k; }
#define MFMA(a, b, c) __builtin_amdgcn_mfma_f32_16x16x32_bf16((a), (b), (c), 0, 0, 0)
#define TRREAD(p) __builtin_amdgcn_ds_read_tr16_b64_v4bf16((LAS bf16x4*)(p))
#define CAT8(lo, hi) __builtin_shufflevector((lo), (hi), 0, 1, 2, 3, 4, 5, 6, 7)

constexpr unsigned LDS_TOTAL = LDS_BYTES;
#define TS(slot) do {} while (0)
#define TSC(slot) do {} while (0)
#define TS_INIT() do {} while (0)
#define TS_DUMP() do {} while (0)

template <int NM>
struct OutPrep { unsigned T1, T2, eo, off[(NM + 1) / 2]; };
template <int NM>
__device__ __forceinline__ OutPrep<NM> out_prep(unsigned lb, int col0, int lane, int kvh, int bp) {
  OutPrep<NM> r;
  const int n16 = lane & 15, g = lane >> 4;
  r.eo = lds_ld(lb + L_FLG + 8);
  r.T1 = 2 * r.eo + 3; r.T2 = r.T1 * 0x9E3779B1u;
#pragma unroll
  for (int mt = 0; mt < NM; mt += 2) {
    const int cc = col0 + 16 * (mt + (n16 >> 3)) + 4 * g, j = cc / 36, q = (cc - 36 * j) >> 2;
    const int dbid = (bp << 4) | (j >> 1) | ((j & 1) << 3);
    r.off[mt / 2] = dbid * OM_STRIDE + ((kvh * 9 + q) * 8 + (n16 & 7)) * 16;
    asm volatile("" : "+v"(r.off[mt / 2]));
  }
  return r;
}
template <int NM>
__device__ __forceinline__ void out_stage(LAS char* L, unsigned lb, const bf16x8 (&WF)[8][NM], const OutPrep<NM>& pr, int wave, int lane, int bid, int kvh, int bp,
                                          char* __restrict__ ws, char* __restrict__ out) {
  const int n16 = lane & 15, g = lane >> 4;
  const unsigned T1 = pr.T1, T2 = pr.T2, eo = pr.eo;
  f32x4 acc[NM];
#pragma unroll
  for (int mt = 0; mt < NM; ++mt) acc[mt] = f32x4{0.f, 0.f, 0.f, 0.f};
  LAS char* ob = L + L_QO + ((n16 >> 2) & 1) * 8 * QO_PITCH + (n16 & 3) * QO_PITCH + g * 8;
  bf16x8 B[8];
#pragma unroll
  for (int ks = 0; ks < 8; ++ks) {
    LAS char* a = ob + (ks >> 2) * 4 * QO_PITCH + (ks & 3) * 64;
    B[ks] = CAT8(*(LAS bf16x4*)a, *(LAS bf16x4*)(a + 32));
  }
#pragma unroll
  for (int ks = 0; ks < 8; ++ks)
#pragma unroll
    for (int mt = 0; mt < NM; ++mt) acc[mt] = MFMA(WF[ks][mt], B[ks], acc[mt]);
  TS(24);
  const __amdgpu_buffer_rsrc_t ro = __builtin_amdgcn_make_buffer_rsrc(ws + WS_OM, 0, OM_BYTES, 0x00020000);
#pragma unroll
  for (int mt = 0; mt < NM; mt += 2) {
    const bool two = mt + 1 < NM;
    u32x4 a = __builtin_bit_cast(u32x4, acc[mt]);
    if (two) {
      const u32x4 b = __builtin_bit_cast(u32x4, acc[mt + 1]);
#pragma unroll
      for (int i = 0; i < 4; ++i) a[i] = __builtin_amdgcn_update_dpp(a[i], b[i], 0x118, 0xf, 0xc, false);
    }
    const unsigned r0 = (a[0] + 0x80u) >> 8, r1 = (a[1] + 0x80u) >> 8, r2 = (a[2] + 0x80u) >> 8, r3 = (a[3] + 0x80u) >> 8;
    const unsigned w0 = r0 | (r1 << 24), w1 = (r1 >> 8) | (r2 << 16), w2 = (r2 >> 16) | (r3 << 8);
    const unsigned ck = chk_pair(w0, w1, w2, n16 & 1, T1, T2);
    if (two || n16 < 8) __builtin_amdgcn_raw_buffer_store_b128(u32x4{w0, w1, w2, ck}, ro, pr.off[mt / 2], 0, ST_AUX);
  }
  TS(25);
  lds_st(lb + L_FLG + 80 + wave * 4, 2);
  const int t = wave * 64 + lane;
  if (wave < 3) {
    for (;;) { const u32x4 a = lds_ld4(lb + L_FLG + 80), b = lds_ld4(lb + L_FLG + 96); if (all_ge(a, 2) && all_ge(b, 2)) break; }
    __builtin_amdgcn_s_sleep(PD2);
    const bool act = t < 144;
    const int q = act ? (t >> 4) : 0, n = 2 * ((t >> 2) & 3) + (t & 1), sq = (t >> 1) & 1;
    const unsigned off = bid * OM_STRIDE + ((sq * 8 * 9 + q) * 8 + n) * 16;
    u32x4 v[8];
    unsigned miss = 0xff;
    const unsigned long long t0 = __builtin_amdgcn_s_memrealtime();
    for (;;) {
      asm volatile("" ::: "memory");
#pragma unroll
      for (int s = 0; s < 8; ++s) if (miss & (1u << s)) v[s] = __builtin_amdgcn_raw_buffer_load_b128(ro, off + s * 72 * 16, 0, LD_AUX);
#pragma unroll
      for (int s = 0; s < 8; ++s) if (miss & (1u << s)) {
        const bool ok = v[s][3] == chk_pair(v[s][0], v[s][1], v[s][2], t & 1, T1, T2);
        if (__all(!act || ok)) miss &= ~(1u << s);
      }
      if (!miss) break;
      if ((long long)(__builtin_amdgcn_s_memrealtime() - t0) > SPIN_CAP) break;
      __builtin_amdgcn_s_sleep(PSLEEP);
    }
    TS(26);
    f32x4 sum = {0.f, 0.f, 0.f, 0.f};
#pragma unroll
    for (int s = 0; s < 8; ++s) {
      const unsigned w0 = v[s][0], w1 = v[s][1], w2 = v[s][2];
      sum[0] += __uint_as_float(w0 << 8);
      sum[1] += __uint_as_float(((w0 >> 24) | (w1 << 8)) << 8);
      sum[2] += __uint_as_float(((w1 >> 16) | (w2 << 16)) << 8);
      sum[3] += __uint_as_float(w2 & 0xffffff00u);
    }
#pragma unroll
    for (int i = 0; i < 4; ++i) sum[i] += dppf<0x4E>(sum[i]);
    if (act && sq == 0) {
      bf16x4 r; r[0] = (__bf16)sum[0]; r[1] = (__bf16)sum[1]; r[2] = (__bf16)sum[2]; r[3] = (__bf16)sum[3];
      *(bf16x4*)(out + ((size_t)((2 * bp + (n >> 2)) * SQ + (n & 3)) * DM + 36 * kvh + 4 * q) * 2) = r;
    }
    if (kvh == 0 && t == 0) __hip_atomic_store((unsigned*)(ws + WS_EO + bp * 128), eo + 1, __ATOMIC_RELAXED, __HIP_MEMORY_SCOPE_AGENT);
  }
}

__global__ __launch_bounds__(512, 2) void attn_kernel(const char* __restrict__ x, const char* __restrict__ Kc, const char* __restrict__ Vc,
                                                       const char* __restrict__ wq, const char* __restrict__ wo, char* __restrict__ out, char* __restrict__ ws) {
  extern __shared__ __attribute__((aligned(16))) char smem[];
  LAS char* const L = (LAS char*)smem;
  const unsigned lb = (unsigned)(uintptr_t)L;
  const int lane = threadIdx.x & 63, wave = __builtin_amdgcn_readfirstlane(threadIdx.x >> 6);
  const int bid = blockIdx.x;
  const int kvh = (bid & 7) * 2 + ((bid >> 3) & 1), bp = bid >> 4;
  const unsigned a_full = lb + L_FLG, a_done = lb + L_FLG + 16, a_bar = lb + L_FLG + 32, a_lf = lb + L_FLG + 48, a_iss = lb + L_FLG + 80, a_park = lb + L_FLG + 112;
  const int n16 = lane & 15, g = lane >> 4;
  const unsigned tstart = (unsigned)__builtin_amdgcn_s_memrealtime();
  TS_INIT();
  TS(0);
  if (wave == 0) { if (lane < 40) lds_st(lb + L_FLG + lane * 4, 0); lds_drain(); }
  __builtin_amdgcn_s_barrier();
  const unsigned vrow = 4 * g + (n16 >> 2);
  const unsigned vad0 = vrow * 256 + ((vrow & 7) << 5) + (((lane & 3) >> 1) << 4) + ((lane & 1) << 3);
  const char* wo_h = wo + (size_t)kvh * 256 * (DM * 2);
  if (wave == 0) {
    __builtin_amdgcn_s_setprio(3);
    const unsigned r4 = lane >> 4, p = lane & 15;
    unsigned ko[4], vo[2];
#pragma unroll
    for (int i = 0; i < 4; ++i) ko[i] = r4 * 256 + ((p ^ ((4 * i + r4) & 15)) << 4);
#pragma unroll
    for (int i = 0; i < 2; ++i) vo[i] = r4 * 256 + ((p ^ (((4 * i + r4) & 7) << 1)) << 4);
    const size_t pair0 = (size_t)((2 * bp) * HKV + kvh) * (SK * DH * 2), pstep = (size_t)HKV * (SK * DH * 2);
    for (;;) { const u32x4 a = lds_ld4(a_iss), b = lds_ld4(a_iss + 16); if (a[0] && a[1] && a[2] && a[3] && b[0] && b[1] && b[2]) break; }
    TS(2);
    unsigned due16 = (tstart + TORG) << 4, min16 = tstart << 4;
    int freeto = N_FREE0;
    int n = 0, pub = 0; unsigned dst = lb, ring_end = lb + NSLOT_A * TILE; int lap = NSLOT_A;
    auto refresh = [&]() {
      bool first = true;
      for (;;) {
        const u32x4 d = lds_ld4(a_done), b = lds_ld4(a_bar);
        int fr = 4 * d[0]; fr = min(fr, (int)(4 * d[1] + 1)); fr = min(fr, (int)(4 * d[2] + 2)); fr = min(fr, (int)(4 * d[3] + 3));
        freeto = all_ge(b, 1) ? fr + lap : N_FREE0;
        if (n < freeto) break;
        if (first) { wait_vm<0>(); pub = n; lds_st(a_full, n); first = false; TS(4); }
        __builtin_amdgcn_s_sleep(1);
      }
    };
    auto publish_landed = [&]() { if (pub < n - 1) { wait_vm<8>(); pub = n - 1; lds_st(a_full, pub); } else __builtin_amdgcn_s_sleep(1); };
#pragma unroll 1
    for (int seg = 0; seg < 4; ++seg) {
      const char* src = ((seg & 1) ? Vc : Kc) + pair0 + (seg >> 1) * pstep;
      const unsigned o0 = (seg & 1) ? vo[0] : ko[0], o1 = (seg & 1) ? vo[1] : ko[1], o2 = (seg & 1) ? vo[0] : ko[2], o3 = (seg & 1) ? vo[1] : ko[3];
      if (seg == 2) { dst = lb; ring_end = lb + NSLOT_B * TILE; freeto -= NSLOT_A - NSLOT_B; lap = NSLOT_B; }
#pragma unroll 1
      for (int t = 0; t < 64; ++t) {
        if (n >= freeto) refresh();
        {
          unsigned now = (unsigned)__builtin_amdgcn_s_memrealtime();
          const unsigned dd = (int)(due16 - min16) > 0 ? due16 : min16;
          while ((int)((now << 4) - dd) < 0) { publish_landed(); now = (unsigned)__builtin_amdgcn_s_memrealtime(); }
          const unsigned lo = (now - TBURST) << 4;
          if ((int)(lo - min16) > 0) min16 = lo;
          min16 += TMIN16; due16 += TAU16;
        }
        wait_vm<8 * (INFL - 1)>();
        dma8(o0, o1, o2, o3, src, src + 4096, dst);
        if (n + 1 - INFL > pub) { pub = n + 1 - INFL; lds_st(a_full, pub); }
        src += TILE; dst += TILE; if (dst == ring_end) dst = lb;
        ++n;
      }
    }
    while (!(lds_ld(a_park) && lds_ld(a_park + 4) && lds_ld(a_park + 8))) {}
    bf16x8 WF[8][6];
    auto rdpark = [&](int k0, int k1) {
#pragma unroll
      for (int ks = k0; ks < k1; ++ks)
#pragma unroll
        for (int mt = 0; mt < 6; ++mt) {
          LAS char* tb = L + L_PARK + ks * TILE + (vad0 ^ (mt << 5));
          WF[ks][mt] = CAT8(TRREAD(tb), TRREAD(tb + 4096));
        }
    };
    static_assert(INFL == 3 || INFL == 4, "drain ladder");
    if (INFL == 4) { wait_vm<24>(); if (pub < NTILE - 3) lds_st(a_full, NTILE - 3); }
    rdpark(0, 3); lds_drain(); wait_vm<16>(); if (pub < NTILE - 2) lds_st(a_full, NTILE - 2);
    rdpark(3, 6); lds_drain(); wait_vm<8>(); lds_st(a_full, NTILE - 1);
    wait_vm<0>(); lds_st(a_full, NTILE);
    TS(1);
    rdpark(6, 8);
    const OutPrep<6> pr = out_prep<6>(lb, 288, lane, kvh, bp);
    lds_drain(); __builtin_amdgcn_s_barrier(); __builtin_amdgcn_s_barrier();
    TS(3);
    out_stage<6>(L, lb, WF, pr, wave, lane, bid, kvh, bp, ws, out);
    TS(6);
    TS_DUMP();
    return;
  }
  if (wave < 4) {
    const int h = wave - 1;
    {
      unsigned wo2[2];
#pragma unroll
      for (int i = 0; i < 2; ++i) { const unsigned ri = lane >> 3, f = ((ri >> 1) & 1) | (i << 1); wo2[i] = ri * (NQ * 2) + (((lane & 7) ^ (f << 1)) << 4); }
      const char* wb = wq + (size_t)kvh * 512 + (bp & 3) * 128 + (size_t)(192 * h) * (NQ * 2);
      const unsigned dst0 = lb + L_W + h * 24 * 1024;
      int ii = 3 * (bp >> 2);
#pragma unroll 1
      for (int i = 0; i < 12; ++i) {
        const char* w1 = wb + (size_t)ii * (16 * NQ * 2); const unsigned dst = dst0 + ii * 2048;
        dma1(wo2[0], w1, dst); dma1(wo2[1], w1 + 8 * NQ * 2, dst + 1024);
        ++ii; if (ii == 12) ii = 0;
      }
      lds_st(a_iss + h * 4, 1);
      if (h == 0) {
        const unsigned eq = read_epoch(ws + WS_EQ + (kvh * 4 + (bp >> 2)) * 128), eo = read_epoch(ws + WS_EO + bp * 128);
        lds_st(a_full + 4, eq); lds_st(a_full + 8, eo);
      }
      TS(1);
      wait_vm<0>(); lds_st(a_lf + h * 4, 1);
      TS(3);
    }
    if (h == 0) {
      while (!all_ge(lds_ld4(lb + L_FLG + 144), 1)) __builtin_amdgcn_s_sleep(16);
#pragma unroll
      for (int it = 0; it < 4; ++it) {
        const int tid = it * 64 + lane, q = tid >> 5, d4 = (tid & 31) * 4;
        f32x4 v[4];
#pragma unroll
        for (int w = 0; w < 4; ++w) v[w] = *(LAS f32x4*)(L + L_SCR + w * SCR_W + q * SCR_PITCH + d4 * 4);
        const f32x4 o = (v[0] + v[1]) + (v[2] + v[3]);
        bf16x4 ob; ob[0] = (__bf16)o[0]; ob[1] = (__bf16)o[1]; ob[2] = (__bf16)o[2]; ob[3] = (__bf16)o[3];
        *(LAS bf16x4*)(L + L_QO + q * QO_PITCH + d4 * 2) = ob;
      }
      lds_drain(); lds_st(lb + L_FLG + 12, 1);
    }
    for (;;) { const u32x4 d = lds_ld4(a_done); if (all_ge(d, 30)) break; __builtin_amdgcn_s_sleep(32); }
    TS(4);
    unsigned wv[2];
    { const unsigned r4 = lane >> 4, p = lane & 15;
#pragma unroll
      for (int i = 0; i < 2; ++i) wv[i] = r4 * (DM * 2) + ((p ^ (((4 * i + r4) & 7) << 1)) << 4); }
    unsigned wdue = tstart + WO_T0 + h * (WO_DT / 3);
    auto ld_tile = [&](const char* src, unsigned dst) {
#pragma unroll 1
      for (int i = 0; i < 8; i += 2) {
        if (WO_DT > 0 && !(i & 2)) {
          while ((int)((unsigned)__builtin_amdgcn_s_memrealtime() - wdue) < 0) __builtin_amdgcn_s_sleep(1);
          wdue += WO_DT;
        }
        dma1w(wv[0], src + (size_t)(4 * i) * (DM * 2), dst + i * 1024); dma1w(wv[1], src + (size_t)(4 * i + 4) * (DM * 2), dst + (i + 1) * 1024);
        if (i & 2) wait_vm<0>();
      }
    };
    bf16x8 WF[8][6];
    const unsigned stg = L_PARK + h * TILE;
#pragma unroll
    for (int ks = 0; ks < 8; ++ks) {
      ld_tile(wo_h + (size_t)(32 * ks) * (DM * 2) + 192 * h, lb + stg);
#pragma unroll
      for (int mt = 0; mt < 6; ++mt) { LAS char* tb = L + stg + (vad0 ^ (mt << 5)); WF[ks][mt] = CAT8(TRREAD(tb), TRREAD(tb + 4096)); }
      lds_drain();
    }
#pragma unroll 1
    for (int ks = h; ks < 8; ks += 3) ld_tile(wo_h + (size_t)(32 * ks) * (DM * 2) + 576, lb + L_PARK + ks * TILE);
    lds_st(a_park + h * 4, 1);
    TS(5);
    const OutPrep<6> pr = out_prep<6>(lb, 96 * h, lane, kvh, bp);
    __builtin_amdgcn_s_barrier(); __builtin_amdgcn_s_barrier();
    TS(6);
    out_stage<6>(L, lb, WF, pr, wave, lane, bid, kvh, bp, ws, out);
    TS(7);
    TS_DUMP(); return;
  }
  const int c = wave - 4;
  unsigned barv = 0;
  auto cbar = [&]() {
    ++barv; lds_drain();
    lds_st(a_bar + c * 4, barv);
    while (!all_ge(lds_ld4(a_bar), barv)) {}
  };
  unsigned full = 0, done = 0;
  auto wait_full = [&](unsigned m) { while (full <= m) { full = lds_ld(a_full); if (full <= m) __builtin_amdgcn_s_sleep(1); } };
  {
    const int rg = bp >> 2, cb = bp & 3, nt = c & 1, ch = c >> 1;
    {
      const char* xb = x + (size_t)(rg * 32) * (DM * 2);
      unsigned dst = lb + L_X + c * 9 * 1024;
      unsigned P = c * 9 * 64 + lane;
#pragma unroll 1
      for (int i = 0; i < 9; ++i) {
        const unsigned n = (P * 58255u) >> 22;
        dma1((P ^ ((n >> 1) & 7)) << 4, xb, dst);
        P += 64; dst += 1024;
      }
      lds_st(a_iss + 12 + c * 4, 1);
    }
    TS(9);
    wait_vm<0>(); lds_st(a_lf + 12 + c * 4, 1);
    TS(10);
    for (;;) { const u32x4 a = lds_ld4(a_lf), b = lds_ld4(a_lf + 16); if (a[0] && a[1] && a[2] && a[3] && b[0] && b[1] && b[2]) break; }
    TS(13);
    const unsigned eq = lds_ld(a_full + 4);
    const unsigned T1 = 2 * eq + 3, T2 = T1 * 0x9E3779B1u;
    const int q4 = n16 >> 2, pp = lane & 3;
    unsigned wad[2][2];
#pragma unroll
    for (int t = 0; t < 2; ++t) { const int row = 8 * g + 4 * t + q4, f = ((row >> 1) & 1) | ((g & 1) << 1);
#pragma unroll
      for (int u = 0; u < 2; ++u) wad[t][u] = L_W + row * 128 + ((f ^ (2 * ch + u)) << 5) + ((pp >> 1) << 4) + ((pp & 1) << 3); }
    f32x4 acc[2] = {}, acb[2] = {};
    const int xn = nt * 16 + n16, xs = (xn >> 1) & 7;
    LAS char* xr0 = L + L_X + (xn * 72 + (g ^ xs)) * 16;
    LAS char* xr1 = L + L_X + (xn * 72 + ((4 + g) ^ xs)) * 16;
    bf16x8 XB[2][2], WA[2][2][2];
    auto rdw = [&](int grp) {
#pragma unroll
      for (int k = 0; k < 2; ++k) {
        XB[grp & 1][k] = *(LAS bf16x8*)((k ? xr1 : xr0) + grp * 128);
#pragma unroll
        for (int u = 0; u < 2; ++u)
          WA[grp & 1][k][u] = CAT8(TRREAD(L + wad[0][u] + (grp * 2 + k) * 4096), TRREAD(L + wad[1][u] + (grp * 2 + k) * 4096));
      }
    };
    rdw(0);
#pragma unroll
    for (int grp = 0; grp < 9; ++grp) {
      __builtin_amdgcn_sched_barrier(0);
      if (grp + 1 < 9) rdw(grp + 1);
      __builtin_amdgcn_sched_barrier(0);
#pragma unroll
      for (int u = 0; u < 2; ++u) { acc[u] = MFMA(WA[grp & 1][0][u], XB[grp & 1][0], acc[u]); acb[u] = MFMA(WA[grp & 1][1][u], XB[grp & 1][1], acb[u]); }
    }
    __builtin_amdgcn_sched_barrier(0);
    acc[0] += acb[0]; acc[1] += acb[1];
    ++barv; lds_drain(); lds_st(a_bar + c * 4, barv);
    TS(11);
    const __amdgpu_buffer_rsrc_t rq = __builtin_amdgcn_make_buffer_rsrc(ws + WS_QM, 0, QM_BYTES, 0x00020000);
    {
      const int dbp = rg * 4 + nt * 2 + (n16 >> 3), dbid = (dbp << 4) | (bid & 15);
#pragma unroll
      for (int u = 0; u < 2; ++u) {
        const f32x4 v = acc[u];
        bf16x4 h; h[0] = (__bf16)v[0]; h[1] = (__bf16)v[1]; h[2] = (__bf16)v[2]; h[3] = (__bf16)v[3];
        const u32x2 w = __builtin_bit_cast(u32x2, h);
        const u32x4 chk = {w[0], w[1], w[0] ^ T1, w[1] ^ T2};
        __builtin_amdgcn_raw_buffer_store_b128(chk, rq, dbid * 8192 + (cb * 128 + (2 * ch + u) * 32 + (n16 & 7) * 4 + g) * 16, 0, QST_AUX);
      }
    }
    TS(12);
    {
      __builtin_amdgcn_s_sleep(PDELAY);
      u32x4 v0, v1; const unsigned off = bid * 8192 + (c * 128 + lane) * 16;
      const unsigned long long t0 = __builtin_amdgcn_s_memrealtime();
      for (;;) {
        asm volatile("" ::: "memory");
        v0 = __builtin_amdgcn_raw_buffer_load_b128(rq, off, 0, QLD_AUX);
        v1 = __builtin_amdgcn_raw_buffer_load_b128(rq, off + 1024, 0, QLD_AUX);
        const bool ok = v0[2] == (v0[0] ^ T1) && v0[3] == (v0[1] ^ T2) && v1[2] == (v1[0] ^ T1) && v1[3] == (v1[1] ^ T2);
        if (__all(ok)) break;
        if ((long long)(__builtin_amdgcn_s_memrealtime() - t0) > SPIN_CAP) break;
        __builtin_amdgcn_s_sleep(2);
      }
      TS(14);
      const int row = (lane >> 2) & 7, gg = lane & 3, r = c >> 1;
      LAS char* qd = L + L_QO + ((row >> 2) * 8 + r * 4 + (row & 3)) * QO_PITCH + (64 * (c & 1) + 16 * (lane >> 5) + 4 * gg) * 2;
      *(LAS u32x2*)(qd) = u32x2{v0[0], v0[1]};
      *(LAS u32x2*)(qd + 64) = u32x2{v1[0], v1[1]};
    }
    cbar();
    if (cb == 0 && c == 0 && lane == 0) __hip_atomic_store((unsigned*)(ws + WS_EQ + (kvh * 4 + rg) * 128), eq + 1, __ATOMIC_RELAXED, __HIP_MEMORY_SCOPE_AGENT);
  }
  TS(2); TSC(20);
  unsigned kad[4];
#pragma unroll
  for (int s = 0; s < 4; ++s) kad[s] = n16 * 256 + (((4 * s + g) ^ n16) << 4);
  const unsigned mlo = n16 < 8 ? 0xffffffffu : 0u;
  int slot = c;
  int mi = c;
  auto next_tile = [&]() -> LAS char* {
    wait_full(mi);
    LAS char* tb = L + slot * TILE;
    const int mn = mi + 4;
    if (mn < N_SW) { slot += 4; if (slot >= NSLOT_A) slot -= NSLOT_A; }
    else if (mi < N_SW) slot = mn - N_SW;
    else { slot += 4; if (slot >= NSLOT_B) slot -= NSLOT_B; }
    mi = mn;
    return tb;
  };
  bf16x8 WF[8][3];
  const int wsb = min(768 + 96 * c, 1024);
  const unsigned wsof = (lane >> 3) * (DM * 2) + (lane & 7) * 16;
  const unsigned wrd = L_SCR + c * SCR_W + vrow * 128 + (768 + 96 * c - wsb) + (((lane & 3) >> 1) << 4) + ((lane & 1) << 3);
  auto wo_issue = [&](int ks) {
    const char* src = wo_h + (size_t)(32 * ks) * (DM * 2) + wsb;
#pragma unroll
    for (int i = 0; i < 4; ++i) dma1w(wsof, src + (size_t)(8 * i) * (DM * 2), lb + L_SCR + c * SCR_W + i * 1024);
  };
  auto wo_read = [&](int ks) {
    wait_vm<0>();
#pragma unroll
    for (int mt = 0; mt < 3; ++mt) WF[ks][mt] = CAT8(TRREAD(L + wrd + mt * 32), TRREAD(L + wrd + mt * 32 + 2048));
    lds_drain();
  };
  auto pair = [&](auto PC) {
    constexpr int p = decltype(PC)::value;
    bf16x8 QA[4], QB[4];
#pragma unroll
    for (int s = 0; s < 4; ++s) {
      const u32x4 v = *(LAS u32x4*)(L + L_QO + (p * 8 + (n16 & 7)) * QO_PITCH + (4 * s + g) * 16);
      QA[s] = __builtin_bit_cast(bf16x8, v & mlo); QB[s] = __builtin_bit_cast(bf16x8, v & ~mlo);
    }
    f32x4 S[8][2];
#pragma unroll
    for (int i = 0; i < 8; ++i) {
      S[i][0] = f32x4{0.f, 0.f, 0.f, 0.f}; S[i][1] = S[i][0];
#pragma unroll
      for (int h = 0; h < 2; ++h) {
        LAS char* tb = next_tile();
        bf16x8 kf[2][4];
#pragma unroll
        for (int rb = 0; rb < 2; ++rb)
#pragma unroll
          for (int s = 0; s < 4; ++s) kf[rb][s] = *(LAS bf16x8*)(tb + kad[s] + rb * 4096);
        ++done; lds_st(a_done + c * 4, done);
#pragma unroll
        for (int s = 0; s < 4; ++s)
#pragma unroll
          for (int rb = 0; rb < 2; ++rb) S[i][rb] = MFMA(kf[rb][s], h ? QB[s] : QA[s], S[i][rb]);
      }
    }
    if (p == 0) TS(3); else TS(6);
    float m = S[0][0][0];
#pragma unroll
    for (int i = 0; i < 8; ++i)
#pragma unroll
      for (int rb = 0; rb < 2; ++rb)
#pragma unroll
        for (int j = 0; j < 4; ++j) m = fmaxf(m, S[i][rb][j]);
    m = fmaxf(m, dppf<0x128>(m));
    m = xrow_max(m);
    const float mc = m * C2;
    float l = 0.f;
    u32x4 P[8];
#pragma unroll
    for (int i = 0; i < 8; ++i) {
      bf16x8 pk;
#pragma unroll
      for (int rb = 0; rb < 2; ++rb)
#pragma unroll
        for (int j = 0; j < 4; ++j) { const float e = __builtin_amdgcn_exp2f(fmaf(S[i][rb][j], C2, -mc)); l += e; pk[rb * 4 + j] = (__bf16)e; }
      P[i] = __builtin_bit_cast(u32x4, pk);
    }
    l += dppf<0x128>(l);
    l = xrow_sum(l);
    if (lane < 8) *(LAS f32x2*)(L + L_ML + p * 256 + (c * 8 + lane) * 8) = f32x2{mc, l};
    lds_st(lb + L_FLG + 128 + c * 4, p + 1);
    float scale = 0.f;
    f32x4 O[8];
#pragma unroll
    for (int cb = 0; cb < 8; ++cb) O[cb] = f32x4{0.f, 0.f, 0.f, 0.f};
#pragma unroll
    for (int i = 0; i < 8; ++i) {
      if (p == 1) { if (i > 0) wo_read(i - 1); else { while (lds_ld(lb + L_FLG + 12) == 0) {} } wo_issue(i); }
#pragma unroll
      for (int h = 0; h < 2; ++h) {
        const bf16x8 pb = __builtin_bit_cast(bf16x8, P[i] & (h ? ~mlo : mlo));
        if (p == 1 && i == 7 && h == 1) wo_read(7);
        if (i == 6 && h == 0) {
          while (!all_ge(lds_ld4(lb + L_FLG + 128), p + 1)) {}
          f32x2 ml[4];
#pragma unroll
          for (int w = 0; w < 4; ++w) ml[w] = *(LAS f32x2*)(L + L_ML + p * 256 + (w * 8 + (n16 & 7)) * 8);
          const float M = fmaxf(fmaxf(ml[0][0], ml[1][0]), fmaxf(ml[2][0], ml[3][0]));
          float Ls = 0.f;
#pragma unroll
          for (int w = 0; w < 4; ++w) Ls = fmaf(__builtin_amdgcn_exp2f(ml[w][0] - M), ml[w][1], Ls);
          scale = __builtin_amdgcn_exp2f(mc - M) / Ls;
        }
        LAS char* tb = next_tile();
        bf16x8 vf[8];
#pragma unroll
        for (int cb = 0; cb < 8; ++cb) vf[cb] = CAT8(TRREAD(tb + (vad0 ^ (cb << 5))), TRREAD(tb + (vad0 ^ (cb << 5)) + 4096));
        ++done; lds_st(a_done + c * 4, done);
#pragma unroll
        for (int cb = 0; cb < 8; ++cb) O[cb] = MFMA(vf[cb], pb, O[cb]);
      }
    }
    if (p == 0) TS(4); else TS(7);
#pragma unroll
    for (int cb = 0; cb < 8; ++cb)
#pragma unroll
      for (int j = 0; j < 4; ++j) { const float t = O[cb][j] * scale; O[cb][j] = t + dppf<0x128>(t); }
    {
      LAS char* dst = L + L_SCR + c * SCR_W + (n16 & 7) * SCR_PITCH + g * 16 + (n16 >> 3) * 256;
#pragma unroll
      for (int cb = 0; cb < 4; ++cb) *(LAS f32x4*)(dst + cb * 64) = (n16 >> 3) ? O[4 + cb] : O[cb];
    }
    if (p == 0) { lds_st(lb + L_FLG + 144 + c * 4, 1); }
    else {
      lds_drain(); __builtin_amdgcn_s_barrier();
      const int tid = c * 64 + lane, q = tid >> 5, d4 = (tid & 31) * 4;
      f32x4 v[4];
#pragma unroll
      for (int w = 0; w < 4; ++w) v[w] = *(LAS f32x4*)(L + L_SCR + w * SCR_W + q * SCR_PITCH + d4 * 4);
      const f32x4 o = (v[0] + v[1]) + (v[2] + v[3]);
      bf16x4 ob; ob[0] = (__bf16)o[0]; ob[1] = (__bf16)o[1]; ob[2] = (__bf16)o[2]; ob[3] = (__bf16)o[3];
      *(LAS bf16x4*)(L + L_QO + (p * 8 + q) * QO_PITCH + d4 * 2) = ob;
      lds_drain(); __builtin_amdgcn_s_barrier();
    }
    if (p == 0) TS(5); else { TS(8); TSC(21); }
  };
  pair(std::integral_constant<int, 0>{});
  const OutPrep<3> pr = out_prep<3>(lb, 384 + 48 * c, lane, kvh, bp);
  pair(std::integral_constant<int, 1>{});
  out_stage<3>(L, lb, WF, pr, wave, lane, bid, kvh, bp, ws, out);
  TS(15);
  TS_DUMP();
}
}

extern "C" void kernel_launch(void* const* d_in, const int* in_sizes, int n_in, void* d_out, int out_size, void* d_ws, size_t ws_size, hipStream_t stream) {
  using namespace gqa;
  (void)hipFuncSetAttribute((const void*)attn_kernel, hipFuncAttributeMaxDynamicSharedMemorySize, LDS_TOTAL);
  hipLaunchKernelGGL(attn_kernel, dim3(256), dim3(512), LDS_TOTAL, stream, (const char*)d_in[0], (const char*)d_in[1], (const char*)d_in[2], (const char*)d_in[3],
                     (const char*)d_in[4], (char*)d_out, (char*)d_ws);
}
```

```cpp
#include <hip/hip_runtime.h>
#include <stdint.h>
#include <type_traits>

namespace gqa {
typedef __bf16 bf16x8 __attribute__((ext_vector_type(8)));
typedef __bf16 bf16x4 __attribute__((ext_vector_type(4)));
typedef __bf16 bf16x2 __attribute__((ext_vector_type(2)));
typedef float f32x4 __attribute__((ext_vector_type(4)));
typedef float f32x2 __attribute__((ext_vector_type(2)));
typedef unsigned u32x4 __attribute__((ext_vector_type(4)));
typedef unsigned u32x2 __attribute__((ext_vector_type(2)));
#define LAS __attribute__((address_space(3)))

constexpr int SQ = 4, DM = 576, HKV = 16, SK = 2048, DH = 128, NQ = 4096;
constexpr int INFL = 4;
constexpr int TAU16 = 448;
constexpr int TMIN16 = 428;
constexpr int TBURST = 60;
constexpr int TORG = 300;
constexpr int WO_T0 = 2500, WO_DT = 145;
constexpr int PDELAY = 28, PD2 = 12, PSLEEP = 8;
constexpr int AUX_X = 18;
constexpr int TILE = 8192, NTILE = 256;
constexpr int NSLOT_A = 17, NSLOT_B = 9, N_SW = 77;
static_assert((N_SW - NSLOT_B) % NSLOT_A == 0, "ring B takes over at a tile whose predecessors sit in slots 0..8");
constexpr unsigned L_W = 4 * TILE, L_X = 13 * TILE;
constexpr unsigned L_PARK = NSLOT_B * TILE;
constexpr int N_FREE0 = 4;
constexpr unsigned L_SCR = NSLOT_A * TILE, SCR_PITCH = 528, SCR_W = 8 * SCR_PITCH;
constexpr unsigned QO_PITCH = 272;
constexpr unsigned L_QO = L_SCR + 17408;
constexpr unsigned L_ML = L_QO + 16 * QO_PITCH;
constexpr unsigned L_FLG = L_ML + 512;
constexpr unsigned LDS_BYTES = L_FLG + 160;
constexpr float C2 = 0.08838834764831845f * 1.4426950408889634f;
constexpr size_t WS_EQ = 0, WS_EO = 64 * 128, WS_QM = 65536, WS_OM = 4u << 20;
constexpr unsigned QM_BYTES = 256 * 8192, OM_STRIDE = 1152 * 16, OM_BYTES = 256 * OM_STRIDE;
constexpr long long SPIN_CAP = 2000000;

__device__ __forceinline__ unsigned lds_ld(unsigned addr) {
  unsigned v; asm volatile("ds_read_b32 %0, %1\n\ts_waitcnt lgkmcnt(0)" : "=v"(v) : "v"(addr) : "memory");
  return __builtin_amdgcn_readfirstlane(v);
}
__device__ __forceinline__ u32x4 lds_ld4(unsigned addr) {
  u32x4 v; asm volatile("ds_read_b128 %0, %1\n\ts_waitcnt lgkmcnt(0)" : "=v"(v) : "v"(addr) : "memory");
  u32x4 r; r[0] = __builtin_amdgcn_readfirstlane(v[0]); r[1] = __builtin_amdgcn_readfirstlane(v[1]);
  r[2] = __builtin_amdgcn_readfirstlane(v[2]); r[3] = __builtin_amdgcn_readfirstlane(v[3]); return r;
}
__device__ __forceinline__ void lds_st(unsigned addr, unsigned val) {
  asm volatile("ds_write_b32 %0, %1" :: "v"(addr), "v"(val) : "memory");
}
__device__ __forceinline__ void lds_drain() { asm volatile("s_waitcnt lgkmcnt(0)" ::: "memory"); }
__device__ __forceinline__ void dma8(unsigned o0, unsigned o1, unsigned o2, unsigned o3, const char* src, const char* src2, unsigned lds) {
  asm volatile("s_mov_b32 m0, %6\n\ts_nop 0\n\t"
               "global_load_lds_dwordx4 %0, %4 nt\n\t"
               "global_load_lds_dwordx4 %1, %4 offset:1024 nt\n\t"
               "global_load_lds_dwordx4 %2, %4 offset:2048 nt\n\t"
               "global_load_lds_dwordx4 %3, %4 offset:3072 nt\n\t"
               "s_add_u32 m0, m0, 0x1000\n\ts_nop 0\n\t"
               "global_load_lds_dwordx4 %0, %5 nt\n\t"
               "global_load_lds_dwordx4 %1, %5 offset:1024 nt\n\t"
               "global_load_lds_dwordx4 %2, %5 offset:2048 nt\n\t"
               "global_load_lds_dwordx4 %3, %5 offset:3072 nt"
               :: "v"(o0), "v"(o1), "v"(o2), "v"(o3), "s"(src), "s"(src2), "s"(lds) : "memory", "scc");
}
__device__ __forceinline__ void dma1(unsigned o, const char* src, unsigned lds) {
  asm volatile("s_mov_b32 m0, %2\n\ts_nop 0\n\tglobal_load_lds_dwordx4 %0, %1 nt" :: "v"(o), "s"(src), "s"(lds) : "memory");
}
template <int N> __device__ __forceinline__ void wait_vm() { asm volatile("s_waitcnt vmcnt(%0)" :: "n"(N) : "memory"); }
template <int CTRL> __device__ __forceinline__ float dppf(float x) {
  return __builtin_bit_cast(float, __builtin_amdgcn_mov_dpp(__builtin_bit_cast(int, x), CTRL, 0xf, 0xf, true));
}
__device__ __forceinline__ float xrow_max(float x) {
  auto s = __builtin_amdgcn_permlane16_swap(__float_as_uint(x), __float_as_uint(x), false, false);
  x = fmaxf(__uint_as_float(s[0]), __uint_as_float(s[1]));
  auto t = __builtin_amdgcn_permlane32_swap(__float_as_uint(x), __float_as_uint(x), false, false);
  return fmaxf(__uint_as_float(t[0]), __uint_as_float(t[1]));
}
__device__ __forceinline__ float xrow_sum(float x) {
  auto s = __builtin_amdgcn_permlane16_swap(__float_as_uint(x), __float_as_uint(x), false, false);
  x = __uint_as_float(s[0]) + __uint_as_float(s[1]);
  auto t = __builtin_amdgcn_permlane32_swap(__float_as_uint(x), __float_as_uint(x), false, false);
  return __uint_as_float(t[0]) + __uint_as_float(t[1]);
}
__device__ __forceinline__ unsigned read_epoch(const char* p) {
  unsigned e; asm volatile("s_load_dword %0, %1, 0x0\n\ts_waitcnt lgkmcnt(0)" : "=s"(e) : "s"(p) : "memory"); return e;
}
__device__ __forceinline__ unsigned rotl32(unsigned x, int r) { return __builtin_amdgcn_alignbit(x, x, 32 - r); }
__device__ __forceinline__ unsigned chk_pair(const unsigned w0, const unsigned w1, const unsigned w2, bool odd, unsigned T1, unsigned T2) {
  const unsigned p0 = __builtin_amdgcn_mov_dpp(w0, 0xB1, 0xf, 0xf, true), p1 = __builtin_amdgcn_mov_dpp(w1, 0xB1, 0xf, 0xf, true), p2 = __builtin_amdgcn_mov_dpp(w2, 0xB1, 0xf, 0xf, true);
  const unsigned a0 = odd ? p0 : w0, a1 = odd ? p1 : w1, a2 = odd ? p2 : w2, b0 = odd ? w0 : p0, b1 = odd ? w1 : p1, b2 = odd ? w2 : p2;
  const unsigned h1 = a0 ^ rotl32(a1, 5) ^ rotl32(a2, 11) ^ rotl32(b0, 17) ^ rotl32(b1, 23) ^ rotl32(b2, 29);
  const unsigned h2 = (rotl32(a0, 3) + rotl32(a1, 9) + rotl32(a2, 15)) ^ (rotl32(b0, 21) + rotl32(b1, 27) + b2);
  return odd ? (h2 ^ T2) : (h1 ^ T1);
}
__device__ __forceinline__ bool all_ge(const u32x4 v, unsigned k) { return v[0] >= k && v[1] >= k && v[2] >= k && v[3] >= k; }
#define MFMA(a, b, c) __builtin_amdgcn_mfma_f32_16x16x32_bf16((a), (b), (c), 0, 0, 0)
#define TRREAD(p) __builtin_amdgcn_ds_read_tr16_b64_v4bf16((LAS bf16x4*)(p))
#define CAT8(lo, hi) __builtin_shufflevector((lo), (hi), 0, 1, 2, 3, 4, 5, 6, 7)


template <int NM>
struct OutPrep { unsigned T1, T2, eo, off[(NM + 1) / 2]; };
template <int NM>
__device__ __forceinline__ OutPrep<NM> out_prep(unsigned lb, int col0, int lane, int kvh, int bp) {
  OutPrep<NM> r;
  const int n16 = lane & 15, g = lane >> 4;
  r.eo = lds_ld(lb + L_FLG + 8);
  r.T1 = 2 * r.eo + 3; r.T2 = r.T1 * 0x9E3779B1u;
#pragma unroll
  for (int mt = 0; mt < NM; mt += 2) {
    const int cc = col0 + 16 * (mt + (n16 >> 3)) + 4 * g, j = cc / 36, q = (cc - 36 * j) >> 2;
    const int dbid = (bp << 4) | (j >> 1) | ((j & 1) << 3);
    r.off[mt / 2] = dbid * OM_STRIDE + ((kvh * 9 + q) * 8 + (n16 & 7)) * 16;
    asm volatile("" : "+v"(r.off[mt / 2]));
  }
  return r;
}
template <int NM>
__device__ __forceinline__ void out_stage(LAS char* L, unsigned lb, const bf16x8 (&WF)[8][NM], const OutPrep<NM>& pr, int wave, int lane, int bid, int kvh, int bp,
                                          char* __restrict__ ws, char* __restrict__ out) {
  const int n16 = lane & 15, g = lane >> 4;
  const unsigned T1 = pr.T1, T2 = pr.T2, eo = pr.eo;
  f32x4 acc[NM];
#pragma unroll
  for (int mt = 0; mt < NM; ++mt) acc[mt] = f32x4{0.f, 0.f, 0.f, 0.f};
  LAS char* ob = L + L_QO + ((n16 >> 2) & 1) * 8 * QO_PITCH + (n16 & 3) * QO_PITCH + g * 8;
  bf16x8 B[8];
#pragma unroll
  for (int ks = 0; ks < 8; ++ks) {
    LAS char* a = ob + (ks >> 2) * 4 * QO_PITCH + (ks & 3) * 64;
    B[ks] = CAT8(*(LAS bf16x4*)a, *(LAS bf16x4*)(a + 32));
  }
  const __amdgpu_buffer_rsrc_t ro = __builtin_amdgcn_make_buffer_rsrc(ws + WS_OM, 0, OM_BYTES, 0x00020000);
#pragma unroll
  for (int mt = 0; mt < NM; mt += 2) {
    const bool two = mt + 1 < NM;
#pragma unroll
    for (int ks = 0; ks < 8; ++ks) {
      acc[mt] = MFMA(WF[ks][mt], B[ks], acc[mt]);
      if (two) acc[mt + 1] = MFMA(WF[ks][two ? mt + 1 : mt], B[ks], acc[two ? mt + 1 : mt]);
    }
    u32x4 a = __builtin_bit_cast(u32x4, acc[mt]);
    if (two) {
      const u32x4 b = __builtin_bit_cast(u32x4, acc[two ? mt + 1 : mt]);
#pragma unroll
      for (int i = 0; i < 4; ++i) a[i] = __builtin_amdgcn_update_dpp(a[i], b[i], 0x118, 0xf, 0xc, false);
    }
    const unsigned r0 = (a[0] + 0x80u) >> 8, r1 = (a[1] + 0x80u) >> 8, r2 = (a[2] + 0x80u) >> 8, r3 = (a[3] + 0x80u) >> 8;
    const unsigned w0 = r0 | (r1 << 24), w1 = (r1 >> 8) | (r2 << 16), w2 = (r2 >> 16) | (r3 << 8);
    const unsigned ck = chk_pair(w0, w1, w2, n16 & 1, T1, T2);
    if (two || n16 < 8) __builtin_amdgcn_raw_buffer_store_b128(u32x4{w0, w1, w2, ck}, ro, pr.off[mt / 2], 0, AUX_X);
  }
  lds_st(lb + L_FLG + 80 + wave * 4, 2);
  const int t = wave * 64 + lane;
  if (wave < 3) {
    for (;;) { const u32x4 a = lds_ld4(lb + L_FLG + 80), b = lds_ld4(lb + L_FLG + 96); if (all_ge(a, 2) && all_ge(b, 2)) break; }
    __builtin_amdgcn_s_sleep(PD2);
    const bool act = t < 144;
    const int q = act ? (t >> 4) : 0, n = 2 * ((t >> 2) & 3) + (t & 1), sq = (t >> 1) & 1;
    const unsigned off = bid * OM_STRIDE + ((sq * 8 * 9 + q) * 8 + n) * 16;
    u32x4 v[8];
    unsigned miss = 0xff;
    const unsigned long long t0 = __builtin_amdgcn_s_memrealtime();
    for (;;) {
      asm volatile("" ::: "memory");
#pragma unroll
      for (int s = 0; s < 8; ++s) if (miss & (1u << s)) v[s] = __builtin_amdgcn_raw_buffer_load_b128(ro, off + s * 72 * 16, 0, AUX_X);
#pragma unroll
      for (int s = 0; s < 8; ++s) if (miss & (1u << s)) {
        const bool ok = v[s][3] == chk_pair(v[s][0], v[s][1], v[s][2], t & 1, T1, T2);
        if (__all(!act || ok)) miss &= ~(1u << s);
      }
      if (!miss) break;
      if ((long long)(__builtin_amdgcn_s_memrealtime() - t0) > SPIN_CAP) break;
      __builtin_amdgcn_s_sleep(PSLEEP);
    }
    f32x4 sum = {0.f, 0.f, 0.f, 0.f};
#pragma unroll
    for (int s = 0; s < 8; ++s) {
      const unsigned w0 = v[s][0], w1 = v[s][1], w2 = v[s][2];
      sum[0] += __uint_as_float(w0 << 8);
      sum[1] += __uint_as_float(((w0 >> 24) | (w1 << 8)) << 8);
      sum[2] += __uint_as_float(((w1 >> 16) | (w2 << 16)) << 8);
      sum[3] += __uint_as_float(w2 & 0xffffff00u);
    }
#pragma unroll
    for (int i = 0; i < 4; ++i) sum[i] += dppf<0x4E>(sum[i]);
    if (act && sq == 0) {
      bf16x4 r; r[0] = (__bf16)sum[0]; r[1] = (__bf16)sum[1]; r[2] = (__bf16)sum[2]; r[3] = (__bf16)sum[3];
      *(bf16x4*)(out + ((size_t)((2 * bp + (n >> 2)) * SQ + (n & 3)) * DM + 36 * kvh + 4 * q) * 2) = r;
    }
    if (kvh == 0 && t == 0) __hip_atomic_store((unsigned*)(ws + WS_EO + bp * 128), eo + 1, __ATOMIC_RELAXED, __HIP_MEMORY_SCOPE_AGENT);
  }
}

__global__ __launch_bounds__(512, 2) void attn_kernel(const char* __restrict__ x, const char* __restrict__ Kc, const char* __restrict__ Vc,
                                                       const char* __restrict__ wq, const char* __restrict__ wo, char* __restrict__ out, char* __restrict__ ws) {
  extern __shared__ __attribute__((aligned(16))) char smem[];
  LAS char* const L = (LAS char*)smem;
  const unsigned lb = (unsigned)(uintptr_t)L;
  const int lane = threadIdx.x & 63, wave = __builtin_amdgcn_readfirstlane(threadIdx.x >> 6);
  const int bid = blockIdx.x;
  const int kvh = (bid & 7) * 2 + ((bid >> 3) & 1), bp = bid >> 4;
  const unsigned a_full = lb + L_FLG, a_done = lb + L_FLG + 16, a_bar = lb + L_FLG + 32, a_lf = lb + L_FLG + 48, a_iss = lb + L_FLG + 80, a_park = lb + L_FLG + 112;
  const int n16 = lane & 15, g = lane >> 4;
  const unsigned tstart = (unsigned)__builtin_amdgcn_s_memrealtime();
  if (wave == 0) { if (lane < 40) lds_st(lb + L_FLG + lane * 4, 0); lds_drain(); }
  __builtin_amdgcn_s_barrier();
  const unsigned vrow = 4 * g + (n16 >> 2);
  const unsigned vad0 = vrow * 256 + ((vrow & 7) << 5) + (((lane & 3) >> 1) << 4) + ((lane & 1) << 3);
  const char* wo_h = wo + (size_t)kvh * 256 * (DM * 2);
  if (wave == 0) {
    __builtin_amdgcn_s_setprio(3);
    const unsigned r4 = lane >> 4, p = lane & 15;
    unsigned ko[4], vo[2];
#pragma unroll
    for (int i = 0; i < 4; ++i) ko[i] = r4 * 256 + ((p ^ ((4 * i + r4) & 15)) << 4);
#pragma unroll
    for (int i = 0; i < 2; ++i) vo[i] = r4 * 256 + ((p ^ (((4 * i + r4) & 7) << 1)) << 4);
    const size_t pair0 = (size_t)((2 * bp) * HKV + kvh) * (SK * DH * 2), pstep = (size_t)HKV * (SK * DH * 2);
    for (;;) { const u32x4 a = lds_ld4(a_iss), b = lds_ld4(a_iss + 16); if (a[0] && a[1] && a[2] && a[3] && b[0] && b[1] && b[2]) break; }
    unsigned due16 = (tstart + TORG) << 4, min16 = tstart << 4;
    int freeto = N_FREE0;
    int n = 0, pub = 0; unsigned dst = lb, ring_end = lb + NSLOT_A * TILE; int lap = NSLOT_A;
    auto refresh = [&]() {
      bool first = true;
      for (;;) {
        const u32x4 d = lds_ld4(a_done), b = lds_ld4(a_bar);
        int fr = 4 * d[0]; fr = min(fr, (int)(4 * d[1] + 1)); fr = min(fr, (int)(4 * d[2] + 2)); fr = min(fr, (int)(4 * d[3] + 3));
        freeto = all_ge(b, 1) ? fr + lap : N_FREE0;
        if (n < freeto) break;
        if (first) { wait_vm<0>(); pub = n; lds_st(a_full, n); first = false; }
        __builtin_amdgcn_s_sleep(1);
      }
    };
    auto publish_landed = [&]() { if (pub < n - 1) { wait_vm<8>(); pub = n - 1; lds_st(a_full, pub); } else __builtin_amdgcn_s_sleep(1); };
#pragma unroll 1
    for (int seg = 0; seg < 4; ++seg) {
      const char* src = ((seg & 1) ? Vc : Kc) + pair0 + (seg >> 1) * pstep;
      const unsigned o0 = (seg & 1) ? vo[0] : ko[0], o1 = (seg & 1) ? vo[1] : ko[1], o2 = (seg & 1) ? vo[0] : ko[2], o3 = (seg & 1) ? vo[1] : ko[3];
#pragma unroll 1
      for (int t = 0; t < 64; ++t) {
        if (n == N_SW) { dst = lb; ring_end = lb + NSLOT_B * TILE; freeto -= NSLOT_A - NSLOT_B; lap = NSLOT_B; }
        if (n >= freeto) refresh();
        {
          unsigned now = (unsigned)__builtin_amdgcn_s_memrealtime();
          const unsigned dd = (int)(due16 - min16) > 0 ? due16 : min16;
          while ((int)((now << 4) - dd) < 0) { publish_landed(); now = (unsigned)__builtin_amdgcn_s_memrealtime(); }
          const unsigned lo = (now - TBURST) << 4;
          if ((int)(lo - min16) > 0) min16 = lo;
          min16 += TMIN16; due16 += TAU16;
        }
        wait_vm<8 * (INFL - 1)>();
        dma8(o0, o1, o2, o3, src, src + 4096, dst);
        if (n + 1 - INFL > pub) { pub = n + 1 - INFL; lds_st(a_full, pub); }
        src += TILE; dst += TILE; if (dst == ring_end) dst = lb;
        ++n;
      }
    }
    while (!(lds_ld(a_park) && lds_ld(a_park + 4) && lds_ld(a_park + 8))) {}
    bf16x8 WF[8][6];
    auto rdpark = [&](int k0, int k1) {
#pragma unroll
      for (int ks = k0; ks < k1; ++ks)
#pragma unroll
        for (int mt = 0; mt < 6; ++mt) {
          LAS char* tb = L + L_PARK + ks * TILE + (vad0 ^ (mt << 5));
          WF[ks][mt] = CAT8(TRREAD(tb), TRREAD(tb + 4096));
        }
    };
    static_assert(INFL == 3 || INFL == 4, "drain ladder");
    if (INFL == 4) { wait_vm<24>(); if (pub < NTILE - 3) lds_st(a_full, NTILE - 3); }
    rdpark(0, 3); lds_drain(); wait_vm<16>(); if (pub < NTILE - 2) lds_st(a_full, NTILE - 2);
    rdpark(3, 6); lds_drain(); wait_vm<8>(); lds_st(a_full, NTILE - 1);
    wait_vm<0>(); lds_st(a_full, NTILE);
    rdpark(6, 8);
    const OutPrep<6> pr = out_prep<6>(lb, 288, lane, kvh, bp);
    lds_drain(); __builtin_amdgcn_s_barrier(); __builtin_amdgcn_s_barrier();
    out_stage<6>(L, lb, WF, pr, wave, lane, bid, kvh, bp, ws, out);
    return;
  }
  if (wave < 4) {
    const int h = wave - 1;
    {
      unsigned wo2[2];
#pragma unroll
      for (int i = 0; i < 2; ++i) { const unsigned ri = lane >> 3, f = ((ri >> 1) & 1) | (i << 1); wo2[i] = ri * (NQ * 2) + (((lane & 7) ^ (f << 1)) << 4); }
      const char* wb = wq + (size_t)kvh * 512 + (bp & 3) * 128 + (size_t)(192 * h) * (NQ * 2);
      const unsigned dst0 = lb + L_W + h * 24 * 1024;
      int ii = 3 * (bp >> 2);
#pragma unroll 1
      for (int i = 0; i < 12; ++i) {
        const char* w1 = wb + (size_t)ii * (16 * NQ * 2); const unsigned dst = dst0 + ii * 2048;
        dma1(wo2[0], w1, dst); dma1(wo2[1], w1 + 8 * NQ * 2, dst + 1024);
        ++ii; if (ii == 12) ii = 0;
      }
      lds_st(a_iss + h * 4, 1);
      if (h == 0) {
        const unsigned eq = read_epoch(ws + WS_EQ + (kvh * 4 + (bp >> 2)) * 128), eo = read_epoch(ws + WS_EO + bp * 128);
        lds_st(a_full + 4, eq); lds_st(a_full + 8, eo);
      }
      wait_vm<0>(); lds_st(a_lf + h * 4, 1);
    }
    for (;;) { const u32x4 d = lds_ld4(a_done); if (all_ge(d, ((N_SW - 10) >> 2) + 1)) break; __builtin_amdgcn_s_sleep(32); }
    unsigned wv[2];
    { const unsigned r4 = lane >> 4, p = lane & 15;
#pragma unroll
      for (int i = 0; i < 2; ++i) wv[i] = r4 * (DM * 2) + ((p ^ (((4 * i + r4) & 7) << 1)) << 4); }
    unsigned wdue = tstart + WO_T0 + h * (WO_DT / 3);
    auto ld_tile = [&](const char* src, unsigned dst) {
#pragma unroll 1
      for (int i = 0; i < 8; i += 2) {
        if (!(i & 2)) {
          while ((int)((unsigned)__builtin_amdgcn_s_memrealtime() - wdue) < 0) __builtin_amdgcn_s_sleep(1);
          wdue += WO_DT;
        }
        dma1(wv[0], src + (size_t)(4 * i) * (DM * 2), dst + i * 1024); dma1(wv[1], src + (size_t)(4 * i + 4) * (DM * 2), dst + (i + 1) * 1024);
        if (i & 2) wait_vm<0>();
      }
    };
    bf16x8 WF[8][6];
    const unsigned stg = L_PARK + h * TILE;
#pragma unroll
    for (int ks = 0; ks < 8; ++ks) {
      ld_tile(wo_h + (size_t)(32 * ks) * (DM * 2) + 192 * h, lb + stg);
#pragma unroll
      for (int mt = 0; mt < 6; ++mt) { LAS char* tb = L + stg + (vad0 ^ (mt << 5)); WF[ks][mt] = CAT8(TRREAD(tb), TRREAD(tb + 4096)); }
      lds_drain();
    }
#pragma unroll 1
    for (int ks = h; ks < 8; ks += 3) ld_tile(wo_h + (size_t)(32 * ks) * (DM * 2) + 576, lb + L_PARK + ks * TILE);
    lds_st(a_park + h * 4, 1);
    const OutPrep<6> pr = out_prep<6>(lb, 96 * h, lane, kvh, bp);
    __builtin_amdgcn_s_barrier(); __builtin_amdgcn_s_barrier();
    out_stage<6>(L, lb, WF, pr, wave, lane, bid, kvh, bp, ws, out);
    return;
  }
  const int c = wave - 4;
  unsigned barv = 0;
  auto cbar = [&]() {
    ++barv; lds_drain();
    lds_st(a_bar + c * 4, barv);
    while (!all_ge(lds_ld4(a_bar), barv)) {}
  };
  unsigned full = 0, done = 0;
  auto wait_full = [&](unsigned m) { while (full <= m) { full = lds_ld(a_full); if (full <= m) __builtin_amdgcn_s_sleep(1); } };
  {
    const int rg = bp >> 2, cb = bp & 3, nt = c & 1, ch = c >> 1;
    {
      const char* xb = x + (size_t)(rg * 32) * (DM * 2);
      unsigned dst = lb + L_X + c * 9 * 1024;
      unsigned P = c * 9 * 64 + lane;
#pragma unroll 1
      for (int i = 0; i < 9; ++i) {
        const unsigned n = (P * 58255u) >> 22;
        dma1((P ^ ((n >> 1) & 7)) << 4, xb, dst);
        P += 64; dst += 1024;
      }
      lds_st(a_iss + 12 + c * 4, 1);
    }
    wait_vm<0>(); lds_st(a_lf + 12 + c * 4, 1);
    for (;;) { const u32x4 a = lds_ld4(a_lf), b = lds_ld4(a_lf + 16); if (a[0] && a[1] && a[2] && a[3] && b[0] && b[1] && b[2]) break; }
    const unsigned eq = lds_ld(a_full + 4);
    const unsigned T1 = 2 * eq + 3, T2 = T1 * 0x9E3779B1u;
    const int q4 = n16 >> 2, pp = lane & 3;
    unsigned wad[2][2];
#pragma unroll
    for (int t = 0; t < 2; ++t) { const int row = 8 * g + 4 * t + q4, f = ((row >> 1) & 1) | ((g & 1) << 1);
#pragma unroll
      for (int u = 0; u < 2; ++u) wad[t][u] = L_W + row * 128 + ((f ^ (2 * ch + u)) << 5) + ((pp >> 1) << 4) + ((pp & 1) << 3); }
    f32x4 acc[2] = {}, acb[2] = {};
    const int xn = nt * 16 + n16, xs = (xn >> 1) & 7;
    LAS char* xr0 = L + L_X + (xn * 72 + (g ^ xs)) * 16;
    LAS char* xr1 = L + L_X + (xn * 72 + ((4 + g) ^ xs)) * 16;
    bf16x8 XB[2][2], WA[2][2][2];
    auto rdw = [&](int grp) {
#pragma unroll
      for (int k = 0; k < 2; ++k) {
        XB[grp & 1][k] = *(LAS bf16x8*)((k ? xr1 : xr0) + grp * 128);
#pragma unroll
        for (int u = 0; u < 2; ++u)
          WA[grp & 1][k][u] = CAT8(TRREAD(L + wad[0][u] + (grp * 2 + k) * 4096), TRREAD(L + wad[1][u] + (grp * 2 + k) * 4096));
      }
    };
    rdw(0);
#pragma unroll
    for (int grp = 0; grp < 9; ++grp) {
      __builtin_amdgcn_sched_barrier(0);
      if (grp + 1 < 9) rdw(grp + 1);
      __builtin_amdgcn_sched_barrier(0);
#pragma unroll
      for (int u = 0; u < 2; ++u) { acc[u] = MFMA(WA[grp & 1][0][u], XB[grp & 1][0], acc[u]); acb[u] = MFMA(WA[grp & 1][1][u], XB[grp & 1][1], acb[u]); }
    }
    __builtin_amdgcn_sched_barrier(0);
    acc[0] += acb[0]; acc[1] += acb[1];
    ++barv; lds_drain(); lds_st(a_bar + c * 4, barv);
    const __amdgpu_buffer_rsrc_t rq = __builtin_amdgcn_make_buffer_rsrc(ws + WS_QM, 0, QM_BYTES, 0x00020000);
    {
      const int dbp = rg * 4 + nt * 2 + (n16 >> 3), dbid = (dbp << 4) | (bid & 15);
#pragma unroll
      for (int u = 0; u < 2; ++u) {
        const f32x4 v = acc[u];
        bf16x4 h; h[0] = (__bf16)v[0]; h[1] = (__bf16)v[1]; h[2] = (__bf16)v[2]; h[3] = (__bf16)v[3];
        const u32x2 w = __builtin_bit_cast(u32x2, h);
        const u32x4 chk = {w[0], w[1], w[0] ^ T1, w[1] ^ T2};
        __builtin_amdgcn_raw_buffer_store_b128(chk, rq, dbid * 8192 + (cb * 128 + (2 * ch + u) * 32 + (n16 & 7) * 4 + g) * 16, 0, AUX_X);
      }
    }
    {
      __builtin_amdgcn_s_sleep(PDELAY);
      u32x4 v0, v1; const unsigned off = bid * 8192 + (c * 128 + lane) * 16;
      const unsigned long long t0 = __builtin_amdgcn_s_memrealtime();
      for (;;) {
        asm volatile("" ::: "memory");
        v0 = __builtin_amdgcn_raw_buffer_load_b128(rq, off, 0, AUX_X);
        v1 = __builtin_amdgcn_raw_buffer_load_b128(rq, off + 1024, 0, AUX_X);
        const bool ok = v0[2] == (v0[0] ^ T1) && v0[3] == (v0[1] ^ T2) && v1[2] == (v1[0] ^ T1) && v1[3] == (v1[1] ^ T2);
        if (__all(ok)) break;
        if ((long long)(__builtin_amdgcn_s_memrealtime() - t0) > SPIN_CAP) break;
        __builtin_amdgcn_s_sleep(2);
      }
      const int row = (lane >> 2) & 7, gg = lane & 3, r = c >> 1;
      LAS char* qd = L + L_QO + ((row >> 2) * 8 + r * 4 + (row & 3)) * QO_PITCH + (64 * (c & 1) + 16 * (lane >> 5) + 4 * gg) * 2;
      *(LAS u32x2*)(qd) = u32x2{v0[0], v0[1]};
      *(LAS u32x2*)(qd + 64) = u32x2{v1[0], v1[1]};
    }
    cbar();
    if (cb == 0 && c == 0 && lane == 0) __hip_atomic_store((unsigned*)(ws + WS_EQ + (kvh * 4 + rg) * 128), eq + 1, __ATOMIC_RELAXED, __HIP_MEMORY_SCOPE_AGENT);
  }
  unsigned kad[4];
#pragma unroll
  for (int s = 0; s < 4; ++s) kad[s] = n16 * 256 + (((4 * s + g) ^ n16) << 4);
  const unsigned mlo = n16 < 8 ? 0xffffffffu : 0u;
  int slot = c;
  int mi = c;
  auto next_tile = [&]() -> LAS char* {
    wait_full(mi);
    LAS char* tb = L + slot * TILE;
    const int mn = mi + 4;
    if (mn < N_SW) { slot += 4; if (slot >= NSLOT_A) slot -= NSLOT_A; }
    else if (mi < N_SW) slot = mn - N_SW;
    else { slot += 4; if (slot >= NSLOT_B) slot -= NSLOT_B; }
    mi = mn;
    return tb;
  };
  bf16x8 WF[8][3];
  const int wsb = min(768 + 96 * c, 1024);
  const unsigned wsof = (lane >> 3) * (DM * 2) + (lane & 7) * 16;
  const unsigned wrd = L_SCR + c * SCR_W + vrow * 128 + (768 + 96 * c - wsb) + (((lane & 3) >> 1) << 4) + ((lane & 1) << 3);
  auto wo_issue = [&](int ks) {
    const char* src = wo_h + (size_t)(32 * ks) * (DM * 2) + wsb;
#pragma unroll
    for (int i = 0; i < 4; ++i) dma1(wsof, src + (size_t)(8 * i) * (DM * 2), lb + L_SCR + c * SCR_W + i * 1024);
  };
  auto wo_read = [&](int ks) {
    wait_vm<0>();
#pragma unroll
    for (int mt = 0; mt < 3; ++mt) WF[ks][mt] = CAT8(TRREAD(L + wrd + mt * 32), TRREAD(L + wrd + mt * 32 + 2048));
    lds_drain();
  };
  auto pair = [&](auto PC) {
    constexpr int p = decltype(PC)::value;
    bf16x8 QA[4], QB[4];
#pragma unroll
    for (int s = 0; s < 4; ++s) {
      const u32x4 v = *(LAS u32x4*)(L + L_QO + (p * 8 + (n16 & 7)) * QO_PITCH + (4 * s + g) * 16);
      QA[s] = __builtin_bit_cast(bf16x8, v & mlo); QB[s] = __builtin_bit_cast(bf16x8, v & ~mlo);
    }
    f32x4 S[8][2];
#pragma unroll
    for (int i = 0; i < 8; ++i) {
      S[i][0] = f32x4{0.f, 0.f, 0.f, 0.f}; S[i][1] = S[i][0];
#pragma unroll
      for (int h = 0; h < 2; ++h) {
        if (p == 1 && i == 1 && h == 0 && c == 0) {
          while (!all_ge(lds_ld4(lb + L_FLG + 144), 1)) {}
#pragma unroll
          for (int it = 0; it < 4; ++it) {
            const int tid = it * 64 + lane, q = tid >> 5, d4 = (tid & 31) * 4;
            f32x4 v[4];
#pragma unroll
            for (int w = 0; w < 4; ++w) v[w] = *(LAS f32x4*)(L + L_SCR + w * SCR_W + q * SCR_PITCH + d4 * 4);
            const f32x4 o = (v[0] + v[1]) + (v[2] + v[3]);
            bf16x4 ob; ob[0] = (__bf16)o[0]; ob[1] = (__bf16)o[1]; ob[2] = (__bf16)o[2]; ob[3] = (__bf16)o[3];
            *(LAS bf16x4*)(L + L_QO + q * QO_PITCH + d4 * 2) = ob;
          }
          lds_drain(); lds_st(lb + L_FLG + 12, 1);
        }
        LAS char* tb = next_tile();
        bf16x8 kf[2][4];
#pragma unroll
        for (int rb = 0; rb < 2; ++rb)
#pragma unroll
          for (int s = 0; s < 4; ++s) kf[rb][s] = *(LAS bf16x8*)(tb + kad[s] + rb * 4096);
        ++done; lds_st(a_done + c * 4, done);
#pragma unroll
        for (int s = 0; s < 4; ++s)
#pragma unroll
          for (int rb = 0; rb < 2; ++rb) S[i][rb] = MFMA(kf[rb][s], h ? QB[s] : QA[s], S[i][rb]);
      }
    }
    float m = S[0][0][0];
#pragma unroll
    for (int i = 0; i < 8; ++i)
#pragma unroll
      for (int rb = 0; rb < 2; ++rb)
#pragma unroll
        for (int j = 0; j < 4; ++j) m = fmaxf(m, S[i][rb][j]);
    m = fmaxf(m, dppf<0x128>(m));
    m = xrow_max(m);
    const float mc = m * C2;
    float l = 0.f;
    u32x4 P[8];
#pragma unroll
    for (int i = 0; i < 8; ++i) {
      bf16x8 pk;
#pragma unroll
      for (int rb = 0; rb < 2; ++rb)
#pragma unroll
        for (int j = 0; j < 4; ++j) { const float e = __builtin_amdgcn_exp2f(fmaf(S[i][rb][j], C2, -mc)); l += e; pk[rb * 4 + j] = (__bf16)e; }
      P[i] = __builtin_bit_cast(u32x4, pk);
    }
    l += dppf<0x128>(l);
    l = xrow_sum(l);
    if (lane < 8) *(LAS f32x2*)(L + L_ML + p * 256 + (c * 8 + lane) * 8) = f32x2{mc, l};
    lds_st(lb + L_FLG + 128 + c * 4, p + 1);
    float scale = 0.f;
    f32x4 O[8];
#pragma unroll
    for (int cb = 0; cb < 8; ++cb) O[cb] = f32x4{0.f, 0.f, 0.f, 0.f};
#pragma unroll
    for (int i = 0; i < 8; ++i) {
      if (p == 1) { if (i > 0) wo_read(i - 1); else { while (lds_ld(lb + L_FLG + 12) == 0) {} } wo_issue(i); }
#pragma unroll
      for (int h = 0; h < 2; ++h) {
        const bf16x8 pb = __builtin_bit_cast(bf16x8, P[i] & (h ? ~mlo : mlo));
        if (p == 1 && i == 7 && h == 1) wo_read(7);
        if (i == 6 && h == 0) {
          while (!all_ge(lds_ld4(lb + L_FLG + 128), p + 1)) {}
          f32x2 ml[4];
#pragma unroll
          for (int w = 0; w < 4; ++w) ml[w] = *(LAS f32x2*)(L + L_ML + p * 256 + (w * 8 + (n16 & 7)) * 8);
          const float M = fmaxf(fmaxf(ml[0][0], ml[1][0]), fmaxf(ml[2][0], ml[3][0]));
          float Ls = 0.f;
#pragma unroll
          for (int w = 0; w < 4; ++w) Ls = fmaf(__builtin_amdgcn_exp2f(ml[w][0] - M), ml[w][1], Ls);
          scale = __builtin_amdgcn_exp2f(mc - M) / Ls;
        }
        LAS char* tb = next_tile();
        bf16x8 vf[8];
#pragma unroll
        for (int cb = 0; cb < 8; ++cb) vf[cb] = CAT8(TRREAD(tb + (vad0 ^ (cb << 5))), TRREAD(tb + (vad0 ^ (cb << 5)) + 4096));
        ++done; lds_st(a_done + c * 4, done);
#pragma unroll
        for (int cb = 0; cb < 8; ++cb) O[cb] = MFMA(vf[cb], pb, O[cb]);
      }
    }
#pragma unroll
    for (int cb = 0; cb < 8; ++cb)
#pragma unroll
      for (int j = 0; j < 4; ++j) { const float t = O[cb][j] * scale; O[cb][j] = t + dppf<0x128>(t); }
    {
      LAS char* dst = L + L_SCR + c * SCR_W + (n16 & 7) * SCR_PITCH + g * 16 + (n16 >> 3) * 256;
#pragma unroll
      for (int cb = 0; cb < 4; ++cb) *(LAS f32x4*)(dst + cb * 64) = (n16 >> 3) ? O[4 + cb] : O[cb];
    }
    if (p == 0) { lds_st(lb + L_FLG + 144 + c * 4, 1); }
    else {
      lds_drain(); __builtin_amdgcn_s_barrier();
      const int tid = c * 64 + lane, q = tid >> 5, d4 = (tid & 31) * 4;
      f32x4 v[4];
#pragma unroll
      for (int w = 0; w < 4; ++w) v[w] = *(LAS f32x4*)(L + L_SCR + w * SCR_W + q * SCR_PITCH + d4 * 4);
      const f32x4 o = (v[0] + v[1]) + (v[2] + v[3]);
      bf16x4 ob; ob[0] = (__bf16)o[0]; ob[1] = (__bf16)o[1]; ob[2] = (__bf16)o[2]; ob[3] = (__bf16)o[3];
      *(LAS bf16x4*)(L + L_QO + (p * 8 + q) * QO_PITCH + d4 * 2) = ob;
      lds_drain(); __builtin_amdgcn_s_barrier();
    }
  };
  pair(std::integral_constant<int, 0>{});
  const OutPrep<3> pr = out_prep<3>(lb, 384 + 48 * c, lane, kvh, bp);
  pair(std::integral_constant<int, 1>{});
  out_stage<3>(L, lb, WF, pr, wave, lane, bid, kvh, bp, ws, out);
}
}

extern "C" void kernel_launch(void* const* d_in, const int* in_sizes, int n_in, void* d_out, int out_size, void* d_ws, size_t ws_size, hipStream_t stream) {
  using namespace gqa;
  (void)hipFuncSetAttribute((const void*)attn_kernel, hipFuncAttributeMaxDynamicSharedMemorySize, LDS_BYTES);
  hipLaunchKernelGGL(attn_kernel, dim3(256), dim3(512), LDS_BYTES, stream, (const char*)d_in[0], (const char*)d_in[1], (const char*)d_in[2], (const char*)d_in[3],
                     (const char*)d_in[4], (char*)d_out, (char*)d_ws);
}
```

```cpp
#include <hip/hip_runtime.h>
#include <stdint.h>
#include <type_traits>

namespace gqa {
typedef __bf16 bf16x8 __attribute__((ext_vector_type(8)));
typedef __bf16 bf16x4 __attribute__((ext_vector_type(4)));
typedef __bf16 bf16x2 __attribute__((ext_vector_type(2)));
typedef float f32x4 __attribute__((ext_vector_type(4)));
typedef float f32x2 __attribute__((ext_vector_type(2)));
typedef unsigned u32x4 __attribute__((ext_vector_type(4)));
typedef unsigned u32x2 __attribute__((ext_vector_type(2)));
#define LAS __attribute__((address_space(3)))

constexpr int SQ = 4, DM = 576, HKV = 16, SK = 2048, DH = 128, NQ = 4096;
constexpr int INFL = 4;
constexpr int TAU16 = 456;
constexpr int TMIN16 = 428;
constexpr int TBURST = 60;
constexpr int BMAX = 300;
constexpr int TORG = 270;
constexpr int WO_T0 = 2500, WO_DT = 145;
constexpr int PDELAY = 28, PD2 = 12, PSLEEP = 8;
constexpr int AUX_X = 18;
constexpr int TILE = 8192, NTILE = 256;
constexpr int NSLOT_A = 17, NSLOT_B = 9, N_SW = 77;
static_assert((N_SW - NSLOT_B) % NSLOT_A == 0, "ring B takes over at a tile whose predecessors sit in slots 0..8");
constexpr unsigned L_W = 4 * TILE, L_X = 13 * TILE;
constexpr unsigned L_PARK = NSLOT_B * TILE;
constexpr int N_FREE0 = 4;
constexpr unsigned L_SCR = NSLOT_A * TILE, SCR_PITCH = 528, SCR_W = 8 * SCR_PITCH;
constexpr unsigned QO_PITCH = 272;
constexpr unsigned L_QO = L_SCR + 17408;
constexpr unsigned L_ML = L_QO + 16 * QO_PITCH;
constexpr unsigned L_FLG = L_ML + 512;
constexpr unsigned LDS_BYTES = L_FLG + 160;
constexpr float C2 = 0.08838834764831845f * 1.4426950408889634f;
constexpr size_t WS_EQ = 0, WS_EO = 64 * 128, WS_QM = 65536, WS_OM = 4u << 20;
constexpr unsigned QM_BYTES = 256 * 8192, OM_STRIDE = 1152 * 16, OM_BYTES = 256 * OM_STRIDE;
constexpr long long SPIN_CAP = 20000000;

__device__ __forceinline__ unsigned lds_ld(unsigned addr) {
  unsigned v; asm volatile("ds_read_b32 %0, %1\n\ts_waitcnt lgkmcnt(0)" : "=v"(v) : "v"(addr) : "memory");
  return __builtin_amdgcn_readfirstlane(v);
}
__device__ __forceinline__ u32x4 lds_ld4(unsigned addr) {
  u32x4 v; asm volatile("ds_read_b128 %0, %1\n\ts_waitcnt lgkmcnt(0)" : "=v"(v) : "v"(addr) : "memory");
  u32x4 r; r[0] = __builtin_amdgcn_readfirstlane(v[0]); r[1] = __builtin_amdgcn_readfirstlane(v[1]);
  r[2] = __builtin_amdgcn_readfirstlane(v[2]); r[3] = __builtin_amdgcn_readfirstlane(v[3]); return r;
}
__device__ __forceinline__ void lds_st(unsigned addr, unsigned val) {
  asm volatile("ds_write_b32 %0, %1" :: "v"(addr), "v"(val) : "memory");
}
__device__ __forceinline__ void lds_drain() { asm volatile("s_waitcnt lgkmcnt(0)" ::: "memory"); }
__device__ __forceinline__ void dma8(unsigned o0, unsigned o1, unsigned o2, unsigned o3, const char* src, const char* src2, unsigned lds) {
  asm volatile("s_mov_b32 m0, %6\n\ts_nop 0\n\t"
               "global_load_lds_dwordx4 %0, %4 nt\n\t"
               "global_load_lds_dwordx4 %1, %4 offset:1024 nt\n\t"
               "global_load_lds_dwordx4 %2, %4 offset:2048 nt\n\t"
               "global_load_lds_dwordx4 %3, %4 offset:3072 nt\n\t"
               "s_add_u32 m0, m0, 0x1000\n\ts_nop 0\n\t"
               "global_load_lds_dwordx4 %0, %5 nt\n\t"
               "global_load_lds_dwordx4 %1, %5 offset:1024 nt\n\t"
               "global_load_lds_dwordx4 %2, %5 offset:2048 nt\n\t"
               "global_load_lds_dwordx4 %3, %5 offset:3072 nt"
               :: "v"(o0), "v"(o1), "v"(o2), "v"(o3), "s"(src), "s"(src2), "s"(lds) : "memory", "scc");
}
__device__ __forceinline__ void dma1(unsigned o, const char* src, unsigned lds) {
  asm volatile("s_mov_b32 m0, %2\n\ts_nop 0\n\tglobal_load_lds_dwordx4 %0, %1 nt" :: "v"(o), "s"(src), "s"(lds) : "memory");
}
template <int N> __device__ __forceinline__ void wait_vm() { asm volatile("s_waitcnt vmcnt(%0)" :: "n"(N) : "memory"); }
template <int CTRL> __device__ __forceinline__ float dppf(float x) {
  return __builtin_bit_cast(float, __builtin_amdgcn_mov_dpp(__builtin_bit_cast(int, x), CTRL, 0xf, 0xf, true));
}
__device__ __forceinline__ float xrow_max(float x) {
  auto s = __builtin_amdgcn_permlane16_swap(__float_as_uint(x), __float_as_uint(x), false, false);
  x = fmaxf(__uint_as_float(s[0]), __uint_as_float(s[1]));
  auto t = __builtin_amdgcn_permlane32_swap(__float_as_uint(x), __float_as_uint(x), false, false);
  return fmaxf(__uint_as_float(t[0]), __uint_as_float(t[1]));
}
__device__ __forceinline__ float xrow_sum(float x) {
  auto s = __builtin_amdgcn_permlane16_swap(__float_as_uint(x), __float_as_uint(x), false, false);
  x = __uint_as_float(s[0]) + __uint_as_float(s[1]);
  auto t = __builtin_amdgcn_permlane32_swap(__float_as_uint(x), __float_as_uint(x), false, false);
  return __uint_as_float(t[0]) + __uint_as_float(t[1]);
}
__device__ __forceinline__ unsigned read_epoch(const char* p) {
  unsigned e; asm volatile("s_load_dword %0, %1, 0x0\n\ts_waitcnt lgkmcnt(0)" : "=s"(e) : "s"(p) : "memory"); return e;
}
__device__ __forceinline__ unsigned rotl32(unsigned x, int r) { return __builtin_amdgcn_alignbit(x, x, 32 - r); }
__device__ __forceinline__ unsigned chk_pair(const unsigned w0, const unsigned w1, const unsigned w2, bool odd, unsigned T1, unsigned T2) {
  const unsigned p0 = __builtin_amdgcn_mov_dpp(w0, 0xB1, 0xf, 0xf, true), p1 = __builtin_amdgcn_mov_dpp(w1, 0xB1, 0xf, 0xf, true), p2 = __builtin_amdgcn_mov_dpp(w2, 0xB1, 0xf, 0xf, true);
  const unsigned a0 = odd ? p0 : w0, a1 = odd ? p1 : w1, a2 = odd ? p2 : w2, b0 = odd ? w0 : p0, b1 = odd ? w1 : p1, b2 = odd ? w2 : p2;
  const unsigned h1 = a0 ^ rotl32(a1, 5) ^ rotl32(a2, 11) ^ rotl32(b0, 17) ^ rotl32(b1, 23) ^ rotl32(b2, 29);
  const unsigned h2 = (rotl32(a0, 3) + rotl32(a1, 9) + rotl32(a2, 15)) ^ (rotl32(b0, 21) + rotl32(b1, 27) + b2);
  return odd ? (h2 ^ T2) : (h1 ^ T1);
}
__device__ __forceinline__ bool all_ge(const u32x4 v, unsigned k) { return v[0] >= k && v[1] >= k && v[2] >= k && v[3] >= k; }
#define MFMA(a, b, c) __builtin_amdgcn_mfma_f32_16x16x32_bf16((a), (b), (c), 0, 0, 0)
#define TRREAD(p) __builtin_amdgcn_ds_read_tr16_b64_v4bf16((LAS bf16x4*)(p))
#define CAT8(lo, hi) __builtin_shufflevector((lo), (hi), 0, 1, 2, 3, 4, 5, 6, 7)


template <int NM>
struct OutPrep { unsigned T1, T2, eo, off[(NM + 1) / 2]; };
template <int NM>
__device__ __forceinline__ OutPrep<NM> out_prep(unsigned lb, int col0, int lane, int kvh, int bp) {
  OutPrep<NM> r;
  const int n16 = lane & 15, g = lane >> 4;
  r.eo = lds_ld(lb + L_FLG + 8);
  r.T1 = 2 * r.eo + 3; r.T2 = r.T1 * 0x9E3779B1u;
#pragma unroll
  for (int mt = 0; mt < NM; mt += 2) {
    const int cc = col0 + 16 * (mt + (n16 >> 3)) + 4 * g, j = cc / 36, q = (cc - 36 * j) >> 2;
    const int dbid = (bp << 4) | (j >> 1) | ((j & 1) << 3);
    r.off[mt / 2] = dbid * OM_STRIDE + ((kvh * 9 + q) * 8 + (n16 & 7)) * 16;
    asm volatile("" : "+v"(r.off[mt / 2]));
  }
  return r;
}
template <int NM>
__device__ __forceinline__ void out_stage(LAS char* L, unsigned lb, const bf16x8 (&WF)[8][NM], const OutPrep<NM>& pr, int wave, int lane, int bid, int kvh, int bp,
                                          char* __restrict__ ws, char* __restrict__ out) {
  const int n16 = lane & 15, g = lane >> 4;
  const unsigned T1 = pr.T1, T2 = pr.T2, eo = pr.eo;
  f32x4 acc[NM];
#pragma unroll
  for (int mt = 0; mt < NM; ++mt) acc[mt] = f32x4{0.f, 0.f, 0.f, 0.f};
  LAS char* ob = L + L_QO + ((n16 >> 2) & 1) * 8 * QO_PITCH + (n16 & 3) * QO_PITCH + g * 8;
  bf16x8 B[8];
#pragma unroll
  for (int ks = 0; ks < 8; ++ks) {
    LAS char* a = ob + (ks >> 2) * 4 * QO_PITCH + (ks & 3) * 64;
    B[ks] = CAT8(*(LAS bf16x4*)a, *(LAS bf16x4*)(a + 32));
  }
  const __amdgpu_buffer_rsrc_t ro = __builtin_amdgcn_make_buffer_rsrc(ws + WS_OM, 0, OM_BYTES, 0x00020000);
#pragma unroll
  for (int mt = 0; mt < NM; mt += 2) {
    const bool two = mt + 1 < NM;
#pragma unroll
    for (int ks = 0; ks < 8; ++ks) {
      acc[mt] = MFMA(WF[ks][mt], B[ks], acc[mt]);
      if (two) acc[mt + 1] = MFMA(WF[ks][two ? mt + 1 : mt], B[ks], acc[two ? mt + 1 : mt]);
    }
    u32x4 a = __builtin_bit_cast(u32x4, acc[mt]);
    if (two) {
      const u32x4 b = __builtin_bit_cast(u32x4, acc[two ? mt + 1 : mt]);
#pragma unroll
      for (int i = 0; i < 4; ++i) a[i] = __builtin_amdgcn_update_dpp(a[i], b[i], 0x118, 0xf, 0xc, false);
    }
    const unsigned r0 = (a[0] + 0x80u) >> 8, r1 = (a[1] + 0x80u) >> 8, r2 = (a[2] + 0x80u) >> 8, r3 = (a[3] + 0x80u) >> 8;
    const unsigned w0 = r0 | (r1 << 24), w1 = (r1 >> 8) | (r2 << 16), w2 = (r2 >> 16) | (r3 << 8);
    const unsigned ck = chk_pair(w0, w1, w2, n16 & 1, T1, T2);
    if (two || n16 < 8) __builtin_amdgcn_raw_buffer_store_b128(u32x4{w0, w1, w2, ck}, ro, pr.off[mt / 2], 0, AUX_X);
  }
  lds_st(lb + L_FLG + 80 + wave * 4, 2);
  const int t = wave * 64 + lane;
  if (wave < 3) {
    for (;;) { const u32x4 a = lds_ld4(lb + L_FLG + 80), b = lds_ld4(lb + L_FLG + 96); if (all_ge(a, 2) && all_ge(b, 2)) break; }
    __builtin_amdgcn_s_sleep(PD2);
    const bool act = t < 144;
    const int q = act ? (t >> 4) : 0, n = 2 * ((t >> 2) & 3) + (t & 1), sq = (t >> 1) & 1;
    const unsigned off = bid * OM_STRIDE + ((sq * 8 * 9 + q) * 8 + n) * 16;
    u32x4 v[8];
    unsigned miss = 0xff;
    const unsigned long long t0 = __builtin_amdgcn_s_memrealtime();
    for (;;) {
      asm volatile("" ::: "memory");
#pragma unroll
      for (int s = 0; s < 8; ++s) if (miss & (1u << s)) v[s] = __builtin_amdgcn_raw_buffer_load_b128(ro, off + s * 72 * 16, 0, AUX_X);
#pragma unroll
      for (int s = 0; s < 8; ++s) if (miss & (1u << s)) {
        const bool ok = v[s][3] == chk_pair(v[s][0], v[s][1], v[s][2], t & 1, T1, T2);
        if (__all(!act || ok)) miss &= ~(1u << s);
      }
      if (!miss) break;
      if ((long long)(__builtin_amdgcn_s_memrealtime() - t0) > SPIN_CAP) break;
      __builtin_amdgcn_s_sleep(PSLEEP);
    }
    f32x4 sum = {0.f, 0.f, 0.f, 0.f};
#pragma unroll
    for (int s = 0; s < 8; ++s) {
      const unsigned w0 = v[s][0], w1 = v[s][1], w2 = v[s][2];
      sum[0] += __uint_as_float(w0 << 8);
      sum[1] += __uint_as_float(((w0 >> 24) | (w1 << 8)) << 8);
      sum[2] += __uint_as_float(((w1 >> 16) | (w2 << 16)) << 8);
      sum[3] += __uint_as_float(w2 & 0xffffff00u);
    }
#pragma unroll
    for (int i = 0; i < 4; ++i) sum[i] += dppf<0x4E>(sum[i]);
    if (act && sq == 0) {
      bf16x4 r; r[0] = (__bf16)sum[0]; r[1] = (__bf16)sum[1]; r[2] = (__bf16)sum[2]; r[3] = (__bf16)sum[3];
      *(bf16x4*)(out + ((size_t)((2 * bp + (n >> 2)) * SQ + (n & 3)) * DM + 36 * kvh + 4 * q) * 2) = r;
    }
    if (kvh == 0 && t == 0) __hip_atomic_store((unsigned*)(ws + WS_EO + bp * 128), eo + 1, __ATOMIC_RELAXED, __HIP_MEMORY_SCOPE_AGENT);
  }
}

__global__ __launch_bounds__(512, 2) void attn_kernel(const char* __restrict__ x, const char* __restrict__ Kc, const char* __restrict__ Vc,
                                                       const char* __restrict__ wq, const char* __restrict__ wo, char* __restrict__ out, char* __restrict__ ws) {
  extern __shared__ __attribute__((aligned(16))) char smem[];
  LAS char* const L = (LAS char*)smem;
  const unsigned lb = (unsigned)(uintptr_t)L;
  const int lane = threadIdx.x & 63, wave = __builtin_amdgcn_readfirstlane(threadIdx.x >> 6);
  const int bid = blockIdx.x;
  const int kvh = (bid & 7) * 2 + ((bid >> 3) & 1), bp = bid >> 4;
  const unsigned a_full = lb + L_FLG, a_done = lb + L_FLG + 16, a_bar = lb + L_FLG + 32, a_lf = lb + L_FLG + 48, a_iss = lb + L_FLG + 80, a_park = lb + L_FLG + 112;
  const int n16 = lane & 15, g = lane >> 4;
  const unsigned tstart = (unsigned)__builtin_amdgcn_s_memrealtime();
  if (wave == 0) { if (lane < 40) lds_st(lb + L_FLG + lane * 4, 0); lds_drain(); }
  __builtin_amdgcn_s_barrier();
  const unsigned vrow = 4 * g + (n16 >> 2);
  const unsigned vad0 = vrow * 256 + ((vrow & 7) << 5) + (((lane & 3) >> 1) << 4) + ((lane & 1) << 3);
  const char* wo_h = wo + (size_t)kvh * 256 * (DM * 2);
  if (wave == 0) {
    __builtin_amdgcn_s_setprio(3);
    const unsigned r4 = lane >> 4, p = lane & 15;
    unsigned ko[4], vo[2];
#pragma unroll
    for (int i = 0; i < 4; ++i) ko[i] = r4 * 256 + ((p ^ ((4 * i + r4) & 15)) << 4);
#pragma unroll
    for (int i = 0; i < 2; ++i) vo[i] = r4 * 256 + ((p ^ (((4 * i + r4) & 7) << 1)) << 4);
    const size_t pair0 = (size_t)((2 * bp) * HKV + kvh) * (SK * DH * 2), pstep = (size_t)HKV * (SK * DH * 2);
    for (;;) { const u32x4 a = lds_ld4(a_iss), b = lds_ld4(a_iss + 16); if (a[0] && a[1] && a[2] && a[3] && b[0] && b[1] && b[2]) break; }
    unsigned due16 = (tstart + TORG) << 4, min16 = tstart << 4;
    int freeto = N_FREE0;
    int n = 0, pub = 0; unsigned dst = lb, ring_end = lb + NSLOT_A * TILE; int lap = NSLOT_A;
    auto refresh = [&]() {
      bool first = true;
      for (;;) {
        const u32x4 d = lds_ld4(a_done), b = lds_ld4(a_bar);
        int fr = 4 * d[0]; fr = min(fr, (int)(4 * d[1] + 1)); fr = min(fr, (int)(4 * d[2] + 2)); fr = min(fr, (int)(4 * d[3] + 3));
        freeto = all_ge(b, 1) ? fr + lap : N_FREE0;
        if (n < freeto) break;
        if (first) { wait_vm<0>(); pub = n; lds_st(a_full, n); first = false; }
        __builtin_amdgcn_s_sleep(1);
      }
    };
    auto publish_landed = [&]() { if (pub < n - 1) { wait_vm<8>(); pub = n - 1; lds_st(a_full, pub); } else __builtin_amdgcn_s_sleep(1); };
#pragma unroll 1
    for (int seg = 0; seg < 4; ++seg) {
      const char* src = ((seg & 1) ? Vc : Kc) + pair0 + (seg >> 1) * pstep;
      const unsigned o0 = (seg & 1) ? vo[0] : ko[0], o1 = (seg & 1) ? vo[1] : ko[1], o2 = (seg & 1) ? vo[0] : ko[2], o3 = (seg & 1) ? vo[1] : ko[3];
#pragma unroll 1
      for (int t = 0; t < 64; ++t) {
        if (n == N_SW) { dst = lb; ring_end = lb + NSLOT_B * TILE; freeto -= NSLOT_A - NSLOT_B; lap = NSLOT_B; }
        if (n >= freeto) refresh();
        {
          unsigned now = (unsigned)__builtin_amdgcn_s_memrealtime();
          const unsigned dd = (int)(due16 - min16) > 0 ? due16 : min16;
          while ((int)((now << 4) - dd) < 0) { publish_landed(); now = (unsigned)__builtin_amdgcn_s_memrealtime(); }
          const unsigned lo = (now - TBURST) << 4, lo2 = (now - BMAX) << 4;
          if ((int)(lo - min16) > 0) min16 = lo;
          if ((int)(lo2 - due16) > 0) due16 = lo2;
          min16 += TMIN16; due16 += TAU16;
        }
        wait_vm<8 * (INFL - 1)>();
        dma8(o0, o1, o2, o3, src, src + 4096, dst);
        if (n + 1 - INFL > pub) { pub = n + 1 - INFL; lds_st(a_full, pub); }
        src += TILE; dst += TILE; if (dst == ring_end) dst = lb;
        ++n;
      }
    }
    while (!(lds_ld(a_park) && lds_ld(a_park + 4) && lds_ld(a_park + 8))) {}
    bf16x8 WF[8][6];
    auto rdpark = [&](int k0, int k1) {
#pragma unroll
      for (int ks = k0; ks < k1; ++ks)
#pragma unroll
        for (int mt = 0; mt < 6; ++mt) {
          LAS char* tb = L + L_PARK + ks * TILE + (vad0 ^ (mt << 5));
          WF[ks][mt] = CAT8(TRREAD(tb), TRREAD(tb + 4096));
        }
    };
    static_assert(INFL == 3 || INFL == 4, "drain ladder");
    if (INFL == 4) { wait_vm<24>(); if (pub < NTILE - 3) lds_st(a_full, NTILE - 3); }
    rdpark(0, 3); lds_drain(); wait_vm<16>(); if (pub < NTILE - 2) lds_st(a_full, NTILE - 2);
    rdpark(3, 6); lds_drain(); wait_vm<8>(); lds_st(a_full, NTILE - 1);
    wait_vm<0>(); lds_st(a_full, NTILE);
    rdpark(6, 8);
    const OutPrep<6> pr = out_prep<6>(lb, 288, lane, kvh, bp);
    lds_drain(); __builtin_amdgcn_s_barrier(); __builtin_amdgcn_s_barrier();
    out_stage<6>(L, lb, WF, pr, wave, lane, bid, kvh, bp, ws, out);
    return;
  }
  if (wave < 4) {
    const int h = wave - 1;
    {
      unsigned wo2[2];
#pragma unroll
      for (int i = 0; i < 2; ++i) { const unsigned ri = lane >> 3, f = ((ri >> 1) & 1) | (i << 1); wo2[i] = ri * (NQ * 2) + (((lane & 7) ^ (f << 1)) << 4); }
      const char* wb = wq + (size_t)kvh * 512 + (bp & 3) * 128 + (size_t)(192 * h) * (NQ * 2);
      const unsigned dst0 = lb + L_W + h * 24 * 1024;
      int ii = 3 * (bp >> 2);
#pragma unroll 1
      for (int i = 0; i < 12; ++i) {
        const char* w1 = wb + (size_t)ii * (16 * NQ * 2); const unsigned dst = dst0 + ii * 2048;
        dma1(wo2[0], w1, dst); dma1(wo2[1], w1 + 8 * NQ * 2, dst + 1024);
        ++ii; if (ii == 12) ii = 0;
      }
      lds_st(a_iss + h * 4, 1);
      if (h == 0) {
        const unsigned eq = read_epoch(ws + WS_EQ + (kvh * 4 + (bp >> 2)) * 128), eo = read_epoch(ws + WS_EO + bp * 128);
        lds_st(a_full + 4, eq); lds_st(a_full + 8, eo);
      }
      wait_vm<0>(); lds_st(a_lf + h * 4, 1);
    }
    for (;;) { const u32x4 d = lds_ld4(a_done); if (all_ge(d, ((N_SW - 10) >> 2) + 1)) break; __builtin_amdgcn_s_sleep(32); }
    unsigned wv[2];
    { const unsigned r4 = lane >> 4, p = lane & 15;
#pragma unroll
      for (int i = 0; i < 2; ++i) wv[i] = r4 * (DM * 2) + ((p ^ (((4 * i + r4) & 7) << 1)) << 4); }
    unsigned wdue = tstart + WO_T0 + h * (WO_DT / 3);
    auto ld_tile = [&](const char* src, unsigned dst) {
#pragma unroll 1
      for (int i = 0; i < 8; i += 2) {
        if (!(i & 2)) {
          while ((int)((unsigned)__builtin_amdgcn_s_memrealtime() - wdue) < 0) __builtin_amdgcn_s_sleep(1);
          wdue += WO_DT;
        }
        dma1(wv[0], src + (size_t)(4 * i) * (DM * 2), dst + i * 1024); dma1(wv[1], src + (size_t)(4 * i + 4) * (DM * 2), dst + (i + 1) * 1024);
        if (i & 2) wait_vm<0>();
      }
    };
    bf16x8 WF[8][6];
    const unsigned stg = L_PARK + h * TILE;
#pragma unroll
    for (int ks = 0; ks < 8; ++ks) {
      ld_tile(wo_h + (size_t)(32 * ks) * (DM * 2) + 192 * h, lb + stg);
#pragma unroll
      for (int mt = 0; mt < 6; ++mt) { LAS char* tb = L + stg + (vad0 ^ (mt << 5)); WF[ks][mt] = CAT8(TRREAD(tb), TRREAD(tb + 4096)); }
      lds_drain();
    }
#pragma unroll 1
    for (int ks = h; ks < 8; ks += 3) ld_tile(wo_h + (size_t)(32 * ks) * (DM * 2) + 576, lb + L_PARK + ks * TILE);
    lds_st(a_park + h * 4, 1);
    const OutPrep<6> pr = out_prep<6>(lb, 96 * h, lane, kvh, bp);
    __builtin_amdgcn_s_barrier(); __builtin_amdgcn_s_barrier();
    out_stage<6>(L, lb, WF, pr, wave, lane, bid, kvh, bp, ws, out);
    return;
  }
  const int c = wave - 4;
  unsigned barv = 0;
  auto cbar = [&]() {
    ++barv; lds_drain();
    lds_st(a_bar + c * 4, barv);
    while (!all_ge(lds_ld4(a_bar), barv)) {}
  };
  unsigned full = 0, done = 0;
  auto wait_full = [&](unsigned m) { while (full <= m) { full = lds_ld(a_full); if (full <= m) __builtin_amdgcn_s_sleep(1); } };
  {
    const int rg = bp >> 2, cb = bp & 3, nt = c & 1, ch = c >> 1;
    {
      const char* xb = x + (size_t)(rg * 32) * (DM * 2);
      unsigned dst = lb + L_X + c * 9 * 1024;
      unsigned P = c * 9 * 64 + lane;
#pragma unroll 1
      for (int i = 0; i < 9; ++i) {
        const unsigned n = (P * 58255u) >> 22;
        dma1((P ^ ((n >> 1) & 7)) << 4, xb, dst);
        P += 64; dst += 1024;
      }
      lds_st(a_iss + 12 + c * 4, 1);
    }
    wait_vm<0>(); lds_st(a_lf + 12 + c * 4, 1);
    for (;;) { const u32x4 a = lds_ld4(a_lf), b = lds_ld4(a_lf + 16); if (a[0] && a[1] && a[2] && a[3] && b[0] && b[1] && b[2]) break; }
    const unsigned eq = lds_ld(a_full + 4);
    const unsigned T1 = 2 * eq + 3, T2 = T1 * 0x9E3779B1u;
    const int q4 = n16 >> 2, pp = lane & 3;
    unsigned wad[2][2];
#pragma unroll
    for (int t = 0; t < 2; ++t) { const int row = 8 * g + 4 * t + q4, f = ((row >> 1) & 1) | ((g & 1) << 1);
#pragma unroll
      for (int u = 0; u < 2; ++u) wad[t][u] = L_W + row * 128 + ((f ^ (2 * ch + u)) << 5) + ((pp >> 1) << 4) + ((pp & 1) << 3); }
    f32x4 acc[2] = {}, acb[2] = {};
    const int xn = nt * 16 + n16, xs = (xn >> 1) & 7;
    LAS char* xr0 = L + L_X + (xn * 72 + (g ^ xs)) * 16;
    LAS char* xr1 = L + L_X + (xn * 72 + ((4 + g) ^ xs)) * 16;
    bf16x8 XB[2][2], WA[2][2][2];
    auto rdw = [&](int grp) {
#pragma unroll
      for (int k = 0; k < 2; ++k) {
        XB[grp & 1][k] = *(LAS bf16x8*)((k ? xr1 : xr0) + grp * 128);
#pragma unroll
        for (int u = 0; u < 2; ++u)
          WA[grp & 1][k][u] = CAT8(TRREAD(L + wad[0][u] + (grp * 2 + k) * 4096), TRREAD(L + wad[1][u] + (grp * 2 + k) * 4096));
      }
    };
    rdw(0);
#pragma unroll
    for (int grp = 0; grp < 9; ++grp) {
      __builtin_amdgcn_sched_barrier(0);
      if (grp + 1 < 9) rdw(grp + 1);
      __builtin_amdgcn_sched_barrier(0);
#pragma unroll
      for (int u = 0; u < 2; ++u) { acc[u] = MFMA(WA[grp & 1][0][u], XB[grp & 1][0], acc[u]); acb[u] = MFMA(WA[grp & 1][1][u], XB[grp & 1][1], acb[u]); }
    }
    __builtin_amdgcn_sched_barrier(0);
    acc[0] += acb[0]; acc[1] += acb[1];
    ++barv; lds_drain(); lds_st(a_bar + c * 4, barv);
    const __amdgpu_buffer_rsrc_t rq = __builtin_amdgcn_make_buffer_rsrc(ws + WS_QM, 0, QM_BYTES, 0x00020000);
    {
      const int dbp = rg * 4 + nt * 2 + (n16 >> 3), dbid = (dbp << 4) | (bid & 15);
#pragma unroll
      for (int u = 0; u < 2; ++u) {
        const f32x4 v = acc[u];
        bf16x4 h; h[0] = (__bf16)v[0]; h[1] = (__bf16)v[1]; h[2] = (__bf16)v[2]; h[3] = (__bf16)v[3];
        const u32x2 w = __builtin_bit_cast(u32x2, h);
        const u32x4 chk = {w[0], w[1], w[0] ^ T1, w[1] ^ T2};
        __builtin_amdgcn_raw_buffer_store_b128(chk, rq, dbid * 8192 + (cb * 128 + (2 * ch + u) * 32 + (n16 & 7) * 4 + g) * 16, 0, AUX_X);
      }
    }
    {
      __builtin_amdgcn_s_sleep(PDELAY);
      u32x4 v0, v1; const unsigned off = bid * 8192 + (c * 128 + lane) * 16;
      const unsigned long long t0 = __builtin_amdgcn_s_memrealtime();
      for (;;) {
        asm volatile("" ::: "memory");
        v0 = __builtin_amdgcn_raw_buffer_load_b128(rq, off, 0, AUX_X);
        v1 = __builtin_amdgcn_raw_buffer_load_b128(rq, off + 1024, 0, AUX_X);
        const bool ok = v0[2] == (v0[0] ^ T1) && v0[3] == (v0[1] ^ T2) && v1[2] == (v1[0] ^ T1) && v1[3] == (v1[1] ^ T2);
        if (__all(ok)) break;
        if ((long long)(__builtin_amdgcn_s_memrealtime() - t0) > SPIN_CAP) break;
        __builtin_amdgcn_s_sleep(2);
      }
      const int row = (lane >> 2) & 7, gg = lane & 3, r = c >> 1;
      LAS char* qd = L + L_QO + ((row >> 2) * 8 + r * 4 + (row & 3)) * QO_PITCH + (64 * (c & 1) + 16 * (lane >> 5) + 4 * gg) * 2;
      *(LAS u32x2*)(qd) = u32x2{v0[0], v0[1]};
      *(LAS u32x2*)(qd + 64) = u32x2{v1[0], v1[1]};
    }
    cbar();
    if (cb == 0 && c == 0 && lane == 0) __hip_atomic_store((unsigned*)(ws + WS_EQ + (kvh * 4 + rg) * 128), eq + 1, __ATOMIC_RELAXED, __HIP_MEMORY_SCOPE_AGENT);
  }
  unsigned kad[4];
#pragma unroll
  for (int s = 0; s < 4; ++s) kad[s] = n16 * 256 + (((4 * s + g) ^ n16) << 4);
  const unsigned mlo = n16 < 8 ? 0xffffffffu : 0u;
  int slot = c;
  int mi = c;
  auto next_tile = [&]() -> LAS char* {
    wait_full(mi);
    LAS char* tb = L + slot * TILE;
    const int mn = mi + 4;
    if (mn < N_SW) { slot += 4; if (slot >= NSLOT_A) slot -= NSLOT_A; }
    else if (mi < N_SW) slot = mn - N_SW;
    else { slot += 4; if (slot >= NSLOT_B) slot -= NSLOT_B; }
    mi = mn;
    return tb;
  };
  bf16x8 WF[8][3];
  const int wsb = min(768 + 96 * c, 1024);
  const unsigned wsof = (lane >> 3) * (DM * 2) + (lane & 7) * 16;
  const unsigned wrd = L_SCR + c * SCR_W + vrow * 128 + (768 + 96 * c - wsb) + (((lane & 3) >> 1) << 4) + ((lane & 1) << 3);
  auto wo_issue = [&](int ks) {
    const char* src = wo_h + (size_t)(32 * ks) * (DM * 2) + wsb;
#pragma unroll
    for (int i = 0; i < 4; ++i) dma1(wsof, src + (size_t)(8 * i) * (DM * 2), lb + L_SCR + c * SCR_W + i * 1024);
  };
  auto wo_read = [&](int ks) {
    wait_vm<0>();
#pragma unroll
    for (int mt = 0; mt < 3; ++mt) WF[ks][mt] = CAT8(TRREAD(L + wrd + mt * 32), TRREAD(L + wrd + mt * 32 + 2048));
    lds_drain();
  };
  OutPrep<3> pr;
#pragma unroll 1
  for (int p = 0; p < 2; ++p) {
    if (p == 1) pr = out_prep<3>(lb, 384 + 48 * c, lane, kvh, bp);
    bf16x8 QA[4], QB[4];
#pragma unroll
    for (int s = 0; s < 4; ++s) {
      const u32x4 v = *(LAS u32x4*)(L + L_QO + (p * 8 + (n16 & 7)) * QO_PITCH + (4 * s + g) * 16);
      QA[s] = __builtin_bit_cast(bf16x8, v & mlo); QB[s] = __builtin_bit_cast(bf16x8, v & ~mlo);
    }
    f32x4 S[8][2];
#pragma unroll
    for (int i = 0; i < 8; ++i) {
      S[i][0] = f32x4{0.f, 0.f, 0.f, 0.f}; S[i][1] = S[i][0];
#pragma unroll
      for (int h = 0; h < 2; ++h) {
        if (p == 1 && i == 1 && h == 0 && c == 0) {
          while (!all_ge(lds_ld4(lb + L_FLG + 144), 1)) {}
#pragma unroll
          for (int it = 0; it < 4; ++it) {
            const int tid = it * 64 + lane, q = tid >> 5, d4 = (tid & 31) * 4;
            f32x4 v[4];
#pragma unroll
            for (int w = 0; w < 4; ++w) v[w] = *(LAS f32x4*)(L + L_SCR + w * SCR_W + q * SCR_PITCH + d4 * 4);
            const f32x4 o = (v[0] + v[1]) + (v[2] + v[3]);
            bf16x4 ob; ob[0] = (__bf16)o[0]; ob[1] = (__bf16)o[1]; ob[2] = (__bf16)o[2]; ob[3] = (__bf16)o[3];
            *(LAS bf16x4*)(L + L_QO + q * QO_PITCH + d4 * 2) = ob;
          }
          lds_drain(); lds_st(lb + L_FLG + 12, 1);
        }
        LAS char* tb = next_tile();
        bf16x8 kf[2][4];
#pragma unroll
        for (int rb = 0; rb < 2; ++rb)
#pragma unroll
          for (int s = 0; s < 4; ++s) kf[rb][s] = *(LAS bf16x8*)(tb + kad[s] + rb * 4096);
        ++done; lds_st(a_done + c * 4, done);
#pragma unroll
        for (int s = 0; s < 4; ++s)
#pragma unroll
          for (int rb = 0; rb < 2; ++rb) S[i][rb] = MFMA(kf[rb][s], h ? QB[s] : QA[s], S[i][rb]);
      }
    }
    float m = S[0][0][0];
#pragma unroll
    for (int i = 0; i < 8; ++i)
#pragma unroll
      for (int rb = 0; rb < 2; ++rb)
#pragma unroll
        for (int j = 0; j < 4; ++j) m = fmaxf(m, S[i][rb][j]);
    m = fmaxf(m, dppf<0x128>(m));
    m = xrow_max(m);
    const float mc = m * C2;
    float l = 0.f;
    u32x4 P[8];
#pragma unroll
    for (int i = 0; i < 8; ++i) {
      bf16x8 pk;
#pragma unroll
      for (int rb = 0; rb < 2; ++rb)
#pragma unroll
        for (int j = 0; j < 4; ++j) { const float e = __builtin_amdgcn_exp2f(fmaf(S[i][rb][j], C2, -mc)); l += e; pk[rb * 4 + j] = (__bf16)e; }
      P[i] = __builtin_bit_cast(u32x4, pk);
    }
    l += dppf<0x128>(l);
    l = xrow_sum(l);
    if (lane < 8) *(LAS f32x2*)(L + L_ML + p * 256 + (c * 8 + lane) * 8) = f32x2{mc, l};
    lds_st(lb + L_FLG + 128 + c * 4, p + 1);
    float scale = 0.f;
    f32x4 O[8];
#pragma unroll
    for (int cb = 0; cb < 8; ++cb) O[cb] = f32x4{0.f, 0.f, 0.f, 0.f};
#pragma unroll
    for (int i = 0; i < 8; ++i) {
      if (p == 1) { if (i > 0) wo_read(i - 1); else { while (lds_ld(lb + L_FLG + 12) == 0) {} } wo_issue(i); }
#pragma unroll
      for (int h = 0; h < 2; ++h) {
        const bf16x8 pb = __builtin_bit_cast(bf16x8, P[i] & (h ? ~mlo : mlo));
        if (p == 1 && i == 7 && h == 1) wo_read(7);
        if (i == 6 && h == 0) {
          while (!all_ge(lds_ld4(lb + L_FLG + 128), p + 1)) {}
          f32x2 ml[4];
#pragma unroll
          for (int w = 0; w < 4; ++w) ml[w] = *(LAS f32x2*)(L + L_ML + p * 256 + (w * 8 + (n16 & 7)) * 8);
          const float M = fmaxf(fmaxf(ml[0][0], ml[1][0]), fmaxf(ml[2][0], ml[3][0]));
          float Ls = 0.f;
#pragma unroll
          for (int w = 0; w < 4; ++w) Ls = fmaf(__builtin_amdgcn_exp2f(ml[w][0] - M), ml[w][1], Ls);
          scale = __builtin_amdgcn_exp2f(mc - M) / Ls;
        }
        LAS char* tb = next_tile();
        bf16x8 vf[8];
#pragma unroll
        for (int cb = 0; cb < 8; ++cb) vf[cb] = CAT8(TRREAD(tb + (vad0 ^ (cb << 5))), TRREAD(tb + (vad0 ^ (cb << 5)) + 4096));
        ++done; lds_st(a_done + c * 4, done);
#pragma unroll
        for (int cb = 0; cb < 8; ++cb) O[cb] = MFMA(vf[cb], pb, O[cb]);
      }
    }
#pragma unroll
    for (int cb = 0; cb < 8; ++cb)
#pragma unroll
      for (int j = 0; j < 4; ++j) { const float t = O[cb][j] * scale; O[cb][j] = t + dppf<0x128>(t); }
    {
      LAS char* dst = L + L_SCR + c * SCR_W + (n16 & 7) * SCR_PITCH + g * 16 + (n16 >> 3) * 256;
#pragma unroll
      for (int cb = 0; cb < 4; ++cb) *(LAS f32x4*)(dst + cb * 64) = (n16 >> 3) ? O[4 + cb] : O[cb];
    }
    if (p == 0) { lds_st(lb + L_FLG + 144 + c * 4, 1); }
    else {
      lds_drain(); __builtin_amdgcn_s_barrier();
      const int tid = c * 64 + lane, q = tid >> 5, d4 = (tid & 31) * 4;
      f32x4 v[4];
#pragma unroll
      for (int w = 0; w < 4; ++w) v[w] = *(LAS f32x4*)(L + L_SCR + w * SCR_W + q * SCR_PITCH + d4 * 4);
      const f32x4 o = (v[0] + v[1]) + (v[2] + v[3]);
      bf16x4 ob; ob[0] = (__bf16)o[0]; ob[1] = (__bf16)o[1]; ob[2] = (__bf16)o[2]; ob[3] = (__bf16)o[3];
      *(LAS bf16x4*)(L + L_QO + (p * 8 + q) * QO_PITCH + d4 * 2) = ob;
      lds_drain(); __builtin_amdgcn_s_barrier();
    }
  }
  out_stage<3>(L, lb, WF, pr, wave, lane, bid, kvh, bp, ws, out);
}
}

extern "C" void kernel_launch(void* const* d_in, const int* in_sizes, int n_in, void* d_out, int out_size, void* d_ws, size_t ws_size, hipStream_t stream) {
  using namespace gqa;
  (void)hipFuncSetAttribute((const void*)attn_kernel, hipFuncAttributeMaxDynamicSharedMemorySize, LDS_BYTES);
  hipLaunchKernelGGL(attn_kernel, dim3(256), dim3(512), LDS_BYTES, stream, (const char*)d_in[0], (const char*)d_in[1], (const char*)d_in[2], (const char*)d_in[3],
                     (const char*)d_in[4], (char*)d_out, (char*)d_ws);
}
```
